```python
import torch

def _softmax(x, axis=-1):
    return torch.softmax(x, dim=axis)
def _gelu(x):
    return torch.nn.functional.gelu(x, approximate="tanh")
def _silu(x):
    return torch.nn.functional.silu(x)
def _sigmoid(x):
    return torch.sigmoid(x)
def _layernorm(x, axis=-1, eps=1e-5):
    m = x.mean(dim=axis, keepdim=True)
    v = x.var(dim=axis, keepdim=True, unbiased=False)
    return (x - m) / torch.sqrt(v + eps)
def _scatter_add(tgt, idx, src, axis=0):
    return tgt.clone().index_add_(axis, idx, src)
def _segment_sum(src, seg, n):
    out = torch.zeros((n,) + src.shape[1:], dtype=src.dtype)
    return out.index_add_(0, seg, src)
def _pad(x, pw):
    flat = tuple(v for pair in reversed(pw) for v in pair)
    return torch.nn.functional.pad(x, flat)

kernel_name = 'recurrent_scan__ssm_conv_s4__moe_arch_mod__prompt'

def setup_inputs():

    _w = (lambda a: (lambda t: t.to(torch.bfloat16) if t.is_floating_point() else t)(torch.as_tensor(a)))
    def _A_setup():
        B, L, D, N = 1, 2048, 512, 128
        A_re = -(lambda _x: torch.exp(torch.as_tensor(_x)))((lambda *s: torch.randn(*s, dtype=torch.bfloat16) * 6.4)(D, N))
        A_im = torch.pi * (lambda *s: torch.randn(*s, dtype=torch.bfloat16) * 0.05)(D, N)
        B_re = (lambda *s: torch.randn(*s, dtype=torch.bfloat16) * 0.1)(D, N)
        B_im = (lambda *s: torch.randn(*s, dtype=torch.bfloat16) * 0.1)(D, N)
        C_re = (lambda *s: torch.randn(*s, dtype=torch.bfloat16) * 0.1)(D, N)
        C_im = (lambda *s: torch.randn(*s, dtype=torch.bfloat16) * 0.1)(D, N)
        log_dt = (lambda *s: torch.randn(*s, dtype=torch.bfloat16) * 0.05)(D) * 0.5 - 4.0
        Dskip = (lambda *s: torch.randn(*s, dtype=torch.bfloat16) * 0.05)(D)
        return {
            "u": _w((lambda *s: torch.randn(*s, dtype=torch.bfloat16) * 0.05)(B, L, D)),
            "A_re": _w(A_re),
            "A_im": _w(A_im),
            "B_re": _w(B_re),
            "B_im": _w(B_im),
            "C_re": _w(C_re),
            "C_im": _w(C_im),
            "log_dt": _w(log_dt),
            "Dskip": _w(Dskip),
        }
    _A_in = _A_setup()

    def _B_setup():
        B, S, D, F, C = 1, 2048, 512, 1792, 2
        return {
            "x": _w((lambda *s: torch.randn(*s, dtype=torch.bfloat16) * 0.05)(B, S, D)),
            "w_r": _w((lambda *s: torch.randn(*s, dtype=torch.bfloat16) * 3.2)(D)),
            "w_gate": _w((lambda *s: torch.randn(*s, dtype=torch.bfloat16) * 0.05)(D, F)),
            "w_up": _w((lambda *s: torch.randn(*s, dtype=torch.bfloat16) * 0.05)(D, F)),
            "w_down": _w((lambda *s: torch.randn(*s, dtype=torch.bfloat16) * 0.05)(F, D)),
        }
    _B_in = _B_setup()

    return {
        "A_u": _A_in["u"],
        "A_A_re": _A_in["A_re"],
        "A_A_im": _A_in["A_im"],
        "A_B_re": _A_in["B_re"],
        "A_B_im": _A_in["B_im"],
        "A_C_re": _A_in["C_re"],
        "A_C_im": _A_in["C_im"],
        "A_log_dt": _A_in["log_dt"],
        "A_Dskip": _A_in["Dskip"],
        "B_x": _B_in["x"],
        "B_w_r": _B_in["w_r"],
        "B_w_gate": _B_in["w_gate"],
        "B_w_up": _B_in["w_up"],
        "B_w_down": _B_in["w_down"],
    }

def reference(A_u, A_A_re, A_A_im, A_B_re, A_B_im, A_C_re, A_C_im, A_log_dt, A_Dskip, B_x, B_w_r, B_w_gate, B_w_up, B_w_down):
    def _A_ref(u, A_re, A_im, B_re, B_im, C_re, C_im, log_dt, Dskip):
        Bsz, L, D = u.shape
        dt = torch.exp(log_dt)[:, None]
        A = A_re + 1j * A_im
        Bc = B_re + 1j * B_im
        Cc = C_re + 1j * C_im
        logdA = dt * A
        dB = dt * Bc
        l = (lambda x, dt: x.to(dt))(torch.arange(L), A_re.dtype)
        P = torch.exp(logdA[:, :, None] * l[None, None, :])
        K = torch.einsum("dn,dnl,dn->dl", Cc, P, dB).real
        L2 = 2 * L
        Kp = _pad(K, ((0, 0), (0, L)))
        up = (lambda x, *d: x.permute(*d))(u, 0, 2, 1)
        up = _pad(up, ((0, 0), (0, 0), (0, L)))
        Kf = (lambda x, axes=(-2,-1): torch.fft.rfft2(x.float(), dim=axes))(Kp[:, None, :], axes=(-2, -1))
        Uf = (lambda x, axes=(-2,-1): torch.fft.rfft2(x.float(), dim=axes))(up[:, :, None, :], axes=(-2, -1))
        Yf = Uf * Kf[None, :, :, :]
        y = (lambda x, s, axes=(-2,-1): torch.fft.irfft2(x, s=s, dim=axes).to(torch.bfloat16))(Yf, (1, L2), axes=(-2, -1))[:, :, 0, :L]
        y = (lambda x, *d: x.permute(*d))(y, 0, 2, 1) + u * Dskip[None, None, :]
        return y

    def _B_ref(x, w_r, w_gate, w_up, w_down):
        B, S, D = x.shape
        C = 2
        r = x @ w_r
        idx = torch.argsort(-r, axis=-1)[:, :C]
        idx_e = (lambda x, n, axis: torch.repeat_interleave(x, n, dim=axis))(idx.reshape(B, C, 1), D, axis=2)
        x_sel = (lambda x, i, axis: torch.take_along_dim(x, i, dim=axis))(x, idx_e, axis=1)
        g = _silu(x_sel @ w_gate)
        u = x_sel @ w_up
        y = (g * u) @ w_down
        r_sel = _sigmoid((lambda x, i, axis: torch.take_along_dim(x, i, dim=axis))(r, idx, axis=1))
        y = y * r_sel.reshape(B, C, 1)
        b_off = (torch.arange(B) * S).reshape(B, 1)
        flat_idx = (idx + b_off).reshape(B * C)
        zero = (x * 0.0).reshape(B * S, D)
        upd = _scatter_add(zero, flat_idx, y.reshape(B * C, D), axis=0)
        return x + upd.reshape(B, S, D)

    x = A_u
    xn = _layernorm(x, axis=-1)
    s = _A_ref(xn, A_A_re, A_A_im, A_B_re, A_B_im, A_C_re, A_C_im, A_log_dt, A_Dskip)
    g = _silu(_B_ref(xn, B_w_r, B_w_gate, B_w_up, B_w_down))
    return x + s * g
```

```cpp
#include <hip/hip_runtime.h>
#include <hip/hip_bf16.h>
#include <stdint.h>

#define L_ 2048
#define D_ 512
#define N_ 128
#define F_ 1792

typedef unsigned short u16;
__device__ __forceinline__ float b2f(u16 v) { return __uint_as_float(((unsigned)v) << 16); }
__device__ __forceinline__ u16 f2b(float f) { __hip_bfloat16 h = __float2bfloat16(f); return *reinterpret_cast<u16*>(&h); }
__device__ __forceinline__ float rb(float f) { return b2f(f2b(f)); }

__global__ void k_ln(const u16* x, const u16* wr, u16* xn, float* r) {
  int t = blockIdx.x; int lane = threadIdx.x;
  float v[8]; float s = 0;
  for (int i = 0; i < 8; i++) { v[i] = b2f(x[t * D_ + lane * 8 + i]); s += v[i]; }
  for (int m = 32; m >= 1; m >>= 1) s += __shfl_xor(s, m);
  float mean = s / D_; float q = 0;
  for (int i = 0; i < 8; i++) { float d = v[i] - mean; q += d * d; }
  for (int m = 32; m >= 1; m >>= 1) q += __shfl_xor(q, m);
  float var = q / D_;
  float mb = rb(mean), sb = rb(sqrtf(rb(rb(var) + 1e-5f)));
  float dot = 0;
  for (int i = 0; i < 8; i++) { float a = rb(rb(v[i] - mb) / sb); xn[t * D_ + lane * 8 + i] = f2b(a); dot += a * b2f(wr[lane * 8 + i]); }
  for (int m = 32; m >= 1; m >>= 1) dot += __shfl_xor(dot, m);
  if (lane == 0) r[t] = rb(dot);
}

__global__ void k_kgen(const u16* Are, const u16* Aim, const u16* Bre, const u16* Bim, const u16* Cre, const u16* Cim, const u16* ldt, float* K) {
  int d = blockIdx.x;
  __shared__ float ar[N_], ai[N_], gr[N_], gi[N_];
  float dt = rb(expf(b2f(ldt[d])));
  if (threadIdx.x < N_) {
    int n = threadIdx.x;
    ar[n] = dt * b2f(Are[d * N_ + n]); ai[n] = dt * b2f(Aim[d * N_ + n]);
    float br = dt * b2f(Bre[d * N_ + n]), bi = dt * b2f(Bim[d * N_ + n]);
    float cr = b2f(Cre[d * N_ + n]), ci = b2f(Cim[d * N_ + n]);
    gr[n] = cr * br - ci * bi; gi[n] = cr * bi + ci * br;
  }
  __syncthreads();
  for (int l = threadIdx.x; l < L_; l += blockDim.x) {
    float lf = rb((float)l); float acc = 0;
    for (int n = 0; n < N_; n++) {
      float e = expf(ar[n] * lf); float s, c; sincosf(ai[n] * lf, &s, &c);
      acc += e * (gr[n] * c - gi[n] * s);
    }
    K[d * L_ + l] = acc;
  }
}

__device__ __forceinline__ int2 top2(const float* r) {
  float b0 = -INFINITY, b1 = -INFINITY; int i0 = 0, i1 = 1;
  for (int t = 0; t < L_; t++) { float v = r[t]; if (v > b0) { b1 = b0; i1 = i0; b0 = v; i0 = t; } else if (v > b1) { b1 = v; i1 = t; } }
  return make_int2(i0, i1);
}

__global__ void k_h(const u16* xn, const float* r, const u16* wg, const u16* wu, float* h, int* idx) {
  int f = blockIdx.x * blockDim.x + threadIdx.x;
  int2 ii = top2(r); int i0 = ii.x, i1 = ii.y;
  if (f == 0) { idx[0] = i0; idx[1] = i1; }
  if (f >= F_) return;
  for (int k = 0; k < 2; k++) {
    int t = k ? i1 : i0; float g = 0, u = 0;
    for (int d = 0; d < D_; d++) { float a = b2f(xn[t * D_ + d]); g += a * b2f(wg[d * F_ + f]); u += a * b2f(wu[d * F_ + f]); }
    g = rb(g); u = rb(u); g = rb(g / (1.f + expf(-g)));
    h[k * F_ + f] = rb(g * u);
  }
}
__global__ void k_down(const float* h, const float* r, const int* idx, const u16* wd, float* upd) {
  int d = blockIdx.x * blockDim.x + threadIdx.x;
  for (int k = 0; k < 2; k++) {
    float y = 0; for (int f = 0; f < F_; f++) y += h[k * F_ + f] * b2f(wd[f * D_ + d]);
    y = rb(y); float rs = rb(1.f / (1.f + expf(-r[idx[k]])));
    upd[k * D_ + d] = rb(y * rs);
  }
}
__global__ void k_conv(const u16* x, const u16* xn, const float* K, const u16* Dsk, const float* upd, const int* idx, u16* out) {
  int d = blockIdx.x;
  __shared__ float ks[L_], us[L_];
  for (int l = threadIdx.x; l < L_; l += blockDim.x) { ks[l] = K[d * L_ + l]; us[l] = b2f(xn[l * D_ + d]); }
  __syncthreads();
  float dsk = b2f(Dsk[d]);
  for (int t = threadIdx.x; t < L_; t += blockDim.x) {
    float acc = 0; for (int j = 0; j <= t; j++) acc += ks[j] * us[t - j];
    float y = rb(acc); float s = rb(y + rb(us[t] * dsk));
    float b = us[t]; if (t == idx[0]) b = rb(b + upd[d]); if (t == idx[1]) b = rb(b + upd[D_ + d]);
    float g = rb(b / (1.f + expf(-b)));
    out[t * D_ + d] = f2b(b2f(x[t * D_ + d]) + rb(s * g));
  }
}

extern "C" void kernel_launch(void* const* d_in, const int* in_sizes, int n_in, void* d_out, int out_size, void* d_ws, size_t ws_size, hipStream_t stream) {
  const u16* x = (const u16*)d_in[0];
  char* ws = (char*)d_ws;
  u16* xn = (u16*)ws; float* K = (float*)(ws + (4 << 20)); float* r = (float*)(ws + (12 << 20)); float* h = r + 4096; float* upd = h + 4096; int* idx = (int*)(upd + 4096);
  k_ln<<<L_, 64, 0, stream>>>(x, (const u16*)d_in[10], xn, r);
  k_kgen<<<D_, 256, 0, stream>>>((const u16*)d_in[1], (const u16*)d_in[2], (const u16*)d_in[3], (const u16*)d_in[4], (const u16*)d_in[5], (const u16*)d_in[6], (const u16*)d_in[7], K);
  k_h<<<F_ / 64, 64, 0, stream>>>(xn, r, (const u16*)d_in[11], (const u16*)d_in[12], h, idx);
  k_down<<<D_ / 64, 64, 0, stream>>>(h, r, idx, (const u16*)d_in[13], upd);
  k_conv<<<D_, 256, 0, stream>>>(x, xn, K, (const u16*)d_in[8], upd, idx, (u16*)d_out);
}
```

```cpp
#define PULL_LATE
#include <hip/hip_runtime.h>
#include <hip/hip_bf16.h>
#include <stdint.h>

typedef unsigned short u16;
typedef unsigned int u32;
typedef unsigned long long u64;
typedef float f2 __attribute__((ext_vector_type(2)));
typedef float f4 __attribute__((ext_vector_type(4)));
typedef float f16v __attribute__((ext_vector_type(16)));
typedef u32 u2v __attribute__((ext_vector_type(2)));
typedef u32 u4v __attribute__((ext_vector_type(4)));
typedef __bf16 bf2 __attribute__((ext_vector_type(2)));
typedef __bf16 bf4 __attribute__((ext_vector_type(4)));
typedef __bf16 bf8 __attribute__((ext_vector_type(8)));
typedef short s4v __attribute__((ext_vector_type(4)));
#define LAS __attribute__((address_space(3)))
#define GAS __attribute__((address_space(1)))
#define DEVI __device__ __forceinline__

#define SLP_S 10
#define SLP_X 4
#define SLP_W 20
#define SPIN_CAP (1u << 18)

constexpr int KREG = 14592;
constexpr int TROW = 272;
constexpr int CS = 2112;
constexpr int O_TA = 1120, O_TB = 1248, O_TC = 1376, O_TD = 1504, O_TE = 1632, O_TF = 1792;
constexpr int O_Y = 8448;
constexpr int UB = 4 * KREG;
constexpr int UCH = 4608;
constexpr int FB = UB + 4 * UCH;
constexpr int MB = FB + 49152;
constexpr int M_TAG = MB;
constexpr int M_TOP = MB + 16;
constexpr int M_YF = MB + 64;
constexpr int M_FF = MB + 128;
constexpr int M_AL = MB + 144;
constexpr int M_SEL = MB + 192;
constexpr int M_PART = MB + 256;
constexpr int M_DUM = MB + 1280;
constexpr int LDS_BYTES = MB + 1536;

constexpr u32 W_EP = 0;
constexpr u32 W_ST = 4096;
constexpr u32 W_XB = 65536;
constexpr u32 XBS = 8704;
constexpr u32 W_FB = 4u << 20;
constexpr u32 W_STAMP = 8u << 20;

#define STAMP(i) do {} while (0)
#define STAMP_DECL do {} while (0)
#define STAMP_FLUSH() do {} while (0)
#define CODE_LINES 64

DEVI float bl(u32 v) { return __uint_as_float(v << 16); }
DEVI float bh(u32 v) { return __uint_as_float(v & 0xffff0000u); }
DEVI u32 pk2(float a, float b) { f2 v = {a, b}; return __builtin_bit_cast(u32, __builtin_convertvector(v, bf2)); }
DEVI bf2 asbf2(u32 v) { return __builtin_bit_cast(bf2, v); }
DEVI float rbf(float a) { return bl(pk2(a, a)); }
DEVI f2 cmul(f2 x, f2 y, f2 yr) { return __builtin_elementwise_fma(x.yy, yr, x.xx * y); }
DEVI f2 rot(f2 y) { return (f2){-y.y, y.x}; }
DEVI f2 cexp(float a2, float rev, float l) {
  float e = __builtin_amdgcn_exp2f(a2 * l), p = __builtin_amdgcn_fractf(rev * l);
  return (f2){e * __builtin_amdgcn_cosf(p), e * __builtin_amdgcn_sinf(p)};
}
DEVI float sigm(float v) { return __builtin_amdgcn_rcpf(1.f + __builtin_amdgcn_exp2f(v * -1.44269504f)); }
template <class T> DEVI T lds_ld(u32 a) { return *(const LAS T*)(uintptr_t)a; }
template <class T> DEVI void lds_st(u32 a, T v) { *(LAS T*)(uintptr_t)a = v; }
template <class T> DEVI T lds_ldv(u32 a) { return *(const volatile LAS T*)(uintptr_t)a; }
template <class T> DEVI void lds_stv(u32 a, T v) { *(volatile LAS T*)(uintptr_t)a = v; }
DEVI bf4 trd(u32 a) { return __builtin_amdgcn_ds_read_tr16_b64_v4bf16((LAS bf4*)(uintptr_t)a); }
DEVI bf8 cat(bf4 a, bf4 b) { return __builtin_shufflevector(a, b, 0, 1, 2, 3, 4, 5, 6, 7); }
DEVI f4 mma(bf8 a, bf8 b, f4 c) { return __builtin_amdgcn_mfma_f32_16x16x32_bf16(a, b, c, 0, 0, 0); }
DEVI u32 rl(u32 v, int l) { return __builtin_amdgcn_readlane(v, l); }
#define DPPF(v, ctl, rm) __uint_as_float(__builtin_amdgcn_update_dpp(0u, __float_as_uint(v), ctl, rm, 0xf, true))
#define DPPU(v, ctl, rm) __builtin_amdgcn_update_dpp(0u, v, ctl, rm, 0xf, true)
DEVI u32 umax(u32 a, u32 b) { return a > b ? a : b; }
DEVI u32 umin(u32 a, u32 b) { return a < b ? a : b; }
DEVI u32 wave_umax(u32 v) {
  v = umax(v, DPPU(v, 0x128, 0xf)); v = umax(v, DPPU(v, 0x124, 0xf)); v = umax(v, DPPU(v, 0x122, 0xf)); v = umax(v, DPPU(v, 0x121, 0xf));
  v = umax(v, DPPU(v, 0x142, 0xa)); v = umax(v, DPPU(v, 0x143, 0xc));
  return rl(v, 63);
}
#define MEMF() asm volatile("" ::: "memory")

#define GLD4_SC1(d, vo, base, imm) asm volatile("global_load_dwordx4 %0, %1, %2 offset:%3 sc1" : "=v"(d) : "v"(vo), "s"(base), "i"(imm) : "memory")
#define GLD1_SC1(d, vo, base, imm) asm volatile("global_load_dword %0, %1, %2 offset:%3 sc1" : "=v"(d) : "v"(vo), "s"(base), "i"(imm) : "memory")
#define GLD2_NT(d, vo, base, imm) asm volatile("global_load_dwordx2 %0, %1, %2 offset:%3 nt" : "=v"(d) : "v"(vo), "s"(base), "i"(imm) : "memory")
#define GLD1_NT(d, vo, base) asm volatile("global_load_dword %0, %1, %2 nt" : "=v"(d) : "v"(vo), "s"(base) : "memory")
#define GLDH(d, vo, base) asm volatile("global_load_ushort %0, %1, %2" : "=v"(d) : "v"(vo), "s"(base) : "memory")
#define GLD4(d, vo, base, imm) asm volatile("global_load_dwordx4 %0, %1, %2 offset:%3" : "=v"(d) : "v"(vo), "s"(base), "i"(imm) : "memory")
DEVI void dma16(u32 vo, const void* base, u32 ldsdst) {
  u32 keep;
  asm volatile("s_mov_b32 %0, m0\n\ts_mov_b32 m0, %3\n\ts_nop 0\n\tglobal_load_lds_dwordx4 %1, %2\n\ts_mov_b32 m0, %0" : "=&s"(keep) : "v"(vo), "s"(base), "s"(ldsdst) : "memory");
}
DEVI void dma16x4(u32 v0, u32 v1, u32 v2, u32 v3, const void* base, u32 ldsdst) {
  u32 keep;
  asm volatile("s_mov_b32 %0, m0\n\ts_mov_b32 m0, %6\n\ts_nop 0\n\tglobal_load_lds_dwordx4 %1, %5\n\tglobal_load_lds_dwordx4 %2, %5 offset:1024\n\t"
               "global_load_lds_dwordx4 %3, %5 offset:2048\n\tglobal_load_lds_dwordx4 %4, %5 offset:3072\n\ts_mov_b32 m0, %0"
               : "=&s"(keep) : "v"(v0), "v"(v1), "v"(v2), "v"(v3), "s"(base), "s"(ldsdst) : "memory");
}
DEVI void dma4nt(u32 vo, const void* base, u32 ldsdst) {
  u32 keep;
  asm volatile("s_mov_b32 %0, m0\n\ts_mov_b32 m0, %3\n\ts_nop 0\n\tglobal_load_lds_dword %1, %2 nt\n\ts_mov_b32 m0, %0" : "=&s"(keep) : "v"(vo), "s"(base), "s"(ldsdst) : "memory");
}
DEVI void dma4(u32 vo, const void* base, u32 ldsdst) {
  u32 keep;
  asm volatile("s_mov_b32 %0, m0\n\ts_mov_b32 m0, %3\n\ts_nop 0\n\tglobal_load_lds_dword %1, %2\n\ts_mov_b32 m0, %0" : "=&s"(keep) : "v"(vo), "s"(base), "s"(ldsdst) : "memory");
}

DEVI bf8 band(int lane, int co, int hw) {
  int lo = (lane & 15) + co - hw - 8 * (lane >> 4);
  u32 m = (u32)(((u64)((1u << (2 * hw + 1)) - 1) << (lo + 24)) >> 24);
  u4v r;
#pragma unroll
  for (int i = 0; i < 4; i++) r[i] = ((m >> (2 * i)) & 1u) * 0x3F80u + ((m >> (2 * i + 1)) & 1u) * 0x3F800000u;
  return __builtin_bit_cast(bf8, r);
}

struct Sched {
  int n, nl, nd;
  int kind[128], p0[128], p1[128], ld[128], tile[128];
  int loff[96];
  int dsrc[40];
  int last[4];
  int lcum[129], ccum[129], yat[4];
};
constexpr int PFD = 8;
constexpr int lswap(int k, int n) { return (k ^ 1) < n ? (k ^ 1) : k; }
constexpr int tile_of(int hf, int i) { return hf == 0 ? (i == 0 ? 0 : i == 1 ? 3 : i == 2 ? 4 : 7) : (i == 0 ? 1 : i == 1 ? 2 : i == 2 ? 5 : 6); }
struct Sched2 { Sched s; int dmap[32]; };
constexpr Sched2 make_sched(int hf) {
  Sched2 r{}; Sched& s = r.s;
  for (int i = 0; i < 4; i++) {
    const int q = tile_of(hf, i), pq = i ? tile_of(hf, i - 1) : -1;
    for (int e = 0; e < 6; e++) {
      const bool n2 = (e <= q - 2) && (e >= q - 4) && !((e <= pq - 2) && (e >= pq - 4));
      const bool n3 = (e <= q - 4) && !(e <= pq - 4);
      if (!(n2 || n3)) continue;
      const int l24 = s.nl++, l16 = s.nl++;
      s.loff[l24] = 64 * (8 * e + 7) - 16; s.loff[l16] = 64 * (8 * e + 7);
      const int bsel[6] = {0, 0, 1, 2, 3, 4}, lsel[6] = {l24, l16, l16, l16, l24, l16};
      const int dm[6] = {4 * e, 4 * e + 1, 4 * e + 2, 4 * e + 3, 24 + 2 * e, 24 + 2 * e + 1};
      for (int k = 0; k < 6; k++) {
        if (k < 4 ? !n2 : !n3) continue;
        s.kind[s.n] = 1; s.p0[s.n] = bsel[k]; s.p1[s.n] = s.nd; s.ld[s.n] = lsel[k]; s.tile[s.n] = i;
        r.dmap[dm[k]] = s.nd; s.dsrc[s.nd++] = s.n; s.n++;
      }
    }
    for (int m = 0; m < 17; m++) {
      const int pp = 8 * q - m;
      if (pp < -7) continue;
      s.loff[s.nl] = 64 * (pp + 7);
      s.kind[s.n] = 0; s.p0[s.n] = m; s.ld[s.n] = s.nl++; s.tile[s.n] = i; s.n++;
    }
    for (int a = 2; a <= 4; a++)
      for (int h = 0; h < 2; h++)
        if (q - a >= 0) { s.kind[s.n] = 2; s.p0[s.n] = 2 * (q - a) + h; s.p1[s.n] = 2 * (a - 2) + h; s.ld[s.n] = -1; s.tile[s.n] = i; s.n++; }
    for (int a = 4; a <= 7; a++)
      if (q - a >= 0) { s.kind[s.n] = 2; s.p0[s.n] = 12 + (q - a); s.p1[s.n] = 6 + (a - 4); s.ld[s.n] = -1; s.tile[s.n] = i; s.n++; }
    s.last[i] = s.n - 1;
    s.yat[i] = s.n + 1;
  }
  int nl = PFD, nc = 0, mx = 0;
  s.lcum[0] = nl; s.ccum[0] = 0;
  for (int i = 0; i < s.n; i++) {
    if (s.ld[i] > mx) mx = s.ld[i];
    int slots = 2;
    if (nl < s.nl && nl <= (mx | 1) + PFD - 1) { nl++; slots--; }
    while (slots > 0 && nc < 2 * s.nd && s.dsrc[nc / 2] <= i - 2) { nc++; slots--; }
    if (slots > 0 && nl < s.nl && nl <= (mx | 1) + PFD - 1) { nl++; slots--; }
    s.lcum[i + 1] = nl; s.ccum[i + 1] = nc;
  }
  return r;
}
template <int HF> struct SchedOf { static constexpr Sched2 v = make_sched(HF); };

struct ConvRegs { bf8 An[17], T[10], bands[5]; };
template <int HF, int I>
DEVI void conv_step(u32 ub, u32 yaddr, u32 faddr, const ConvRegs& R, bf8 (&fr)[96], f4 (&D)[40], u32 (&P)[80], f4 (&acc)[4]) {
  constexpr const Sched2& S2 = SchedOf<HF>::v; constexpr const Sched& S = S2.s;
  if constexpr (I < S.n) {
    const f4 z = {0.f, 0.f, 0.f, 0.f};
    constexpr int t = S.tile[I], p0 = S.p0[I], p1 = S.p1[I], ld = S.ld[I];
    if constexpr (S.kind[I] == 0) acc[t] = mma(R.An[p0], fr[ld], p0 == 0 ? z : acc[t]);
    else if constexpr (S.kind[I] == 1) D[p1] = mma(fr[ld], R.bands[p0], z);
    else {
      constexpr int d0 = S2.dmap[2 * S.p0[I]], d1 = S2.dmap[2 * S.p0[I] + 1];
      static_assert(S.ccum[I] >= 2 * d0 + 2 && S.ccum[I] >= 2 * d1 + 2, "window not converted in time");
      acc[t] = mma(__builtin_bit_cast(bf8, ((u4v){P[2 * d0], P[2 * d0 + 1], P[2 * d1], P[2 * d1 + 1]})), R.T[p1], acc[t]);
    }
    __builtin_amdgcn_sched_barrier(0);
    constexpr int l0 = S.lcum[I], l1 = S.lcum[I + 1], c0 = S.ccum[I], c1 = S.ccum[I + 1];
    if constexpr (l0 < l1) { constexpr int k = lswap(l0, S.nl), o = S.loff[k]; fr[k] = lds_ld<bf8>(ub + o); }
    if constexpr (l0 + 1 < l1) { constexpr int k = lswap(l0 + 1, S.nl), o = S.loff[k]; fr[k] = lds_ld<bf8>(ub + o); }
    if constexpr (c0 < c1) P[c0] = pk2(D[c0 / 2][2 * (c0 & 1)], D[c0 / 2][2 * (c0 & 1) + 1]);
    if constexpr (c0 + 1 < c1) P[c0 + 1] = pk2(D[(c0 + 1) / 2][2 * ((c0 + 1) & 1)], D[(c0 + 1) / 2][2 * ((c0 + 1) & 1) + 1]);
    constexpr int yi = S.yat[0] == I ? 0 : S.yat[1] == I ? 1 : S.yat[2] == I ? 2 : I == S.n - 1 ? 3 : -1;
    if constexpr (yi >= 0) {
      const f4 y = acc[yi] + 0.0f;
      asm volatile("ds_write_b128 %0, %1 offset:%2\n\tds_write_b32 %3, %4 offset:%5" :: "v"(yaddr), "v"(y), "i"(yi * 1024), "v"(faddr), "v"(yaddr), "i"(yi * 16));
    }
    __builtin_amdgcn_sched_barrier(0);
    conv_step<HF, I + 1>(ub, yaddr, faddr, R, fr, D, P, acc);
  }
}
template <int HF, int L>
DEVI void conv_pre(u32 ub, bf8 (&fr)[96]) {
  if constexpr (L < PFD) { constexpr int k = lswap(L, 99), o = SchedOf<HF>::v.s.loff[k]; fr[k] = lds_ld<bf8>(ub + o); conv_pre<HF, L + 1>(ub, fr); }
}
template <int HF>
DEVI void conv(u32 ub, u32 yaddr, u32 faddr, const ConvRegs& R) {
  bf8 fr[96]; f4 D[40]; u32 P[80]; f4 acc[4];
  conv_pre<HF, 0>(ub, fr);
  __builtin_amdgcn_sched_barrier(0);
  conv_step<HF, 0>(ub, yaddr, faddr, R, fr, D, P, acc);
}

__global__ void __launch_bounds__(512, 2)
s4_moe_kernel(const u16* __restrict__ x, const u16* __restrict__ Are, const u16* __restrict__ Aim,
              const u16* __restrict__ Bre, const u16* __restrict__ Bim, const u16* __restrict__ Cre,
              const u16* __restrict__ Cim, const u16* __restrict__ ldt, const u16* __restrict__ dsk,
              const u16* __restrict__ wr, const u16* __restrict__ wg, const u16* __restrict__ wu,
              const u16* __restrict__ wd, u16* __restrict__ out, char* __restrict__ ws) {
  extern __shared__ __attribute__((aligned(16))) char smem[];
  const int lane = threadIdx.x & 63;
  const int wid = __builtin_amdgcn_readfirstlane(threadIdx.x >> 6);
  const int w = blockIdx.x, xc = w & 7, mm = w >> 3;
  const int cg = xc * 16 + (mm >> 1), hf = mm & 1;
  const bool prod = mm < 14;
  STAMP_DECL;
  STAMP(0);

  if (wid < 4) {
    __builtin_amdgcn_s_setprio(2);
    const int d = 4 * cg + wid;
    u2v xt0, xt1, xt2, xt3, xt4, xt5, xt6, xt7;
    const int R = xc * 4 + (mm >> 3), S = mm & 7;
    if (wid < 2) {
      const u32 xp = (64 * R + 8 * (lane & 7)) * 1024 + 128 * S + 64 * wid + 8 * (lane >> 3), xq = xp + 4096;
      GLD2_NT(xt0, xp, x, 0); GLD2_NT(xt1, xp, x, 1024); GLD2_NT(xt2, xp, x, 2048); GLD2_NT(xt3, xp, x, 3072);
      GLD2_NT(xt4, xq, x, 0); GLD2_NT(xt5, xq, x, 1024); GLD2_NT(xt6, xq, x, 2048); GLD2_NT(xt7, xq, x, 3072);
    }
    const u32 po = d * 256 + lane * 4;
    u32 p_ar, p_ai, p_br, p_bi, p_cr, p_ci, p_dt;
    GLDH(p_dt, 2 * d, ldt);
    GLD1_NT(p_ar, po, Are); GLD1_NT(p_ai, po, Aim); GLD1_NT(p_br, po, Bre); GLD1_NT(p_bi, po, Bim); GLD1_NT(p_cr, po, Cre); GLD1_NT(p_ci, po, Cim);
    if (wid >= 2) {
      u64 pc; asm volatile("s_getpc_b64 %0" : "=s"(pc));
      const u32 ln = (wid - 2) * 64 + lane;
      dma4((ln < CODE_LINES ? ln : 0) * 128, (const void*)pc, M_DUM);
      dma4(0u, out + 4 * cg, M_DUM);
    }
    if (lane < 32) lds_st<u4v>(UB + wid * UCH + lane * 16, (u4v){0, 0, 0, 0});
    asm volatile("s_waitcnt lgkmcnt(0)\n\ts_barrier" ::: "memory");
    STAMP(6);
    ConvRegs CR;
    CR.bands[0] = band(lane, 8, 2); CR.bands[1] = band(lane, 12, 1); CR.bands[2] = band(lane, 4, 1); CR.bands[3] = band(lane, 8, 4); CR.bands[4] = band(lane, 8, 3);
    const u32 kb = wid * KREG;
    const int hh = lane >> 5, j = lane & 31, od = lane & 1, wh = lane >> 2;
    u32 a0 = kb + 32 + 16 * j + 8 * hh;
    u32 a1 = kb + (lane < 36 ? 544 + 16 * lane : lane < 38 ? 16 * (lane - 36) : O_Y);
    u32 tA = kb + (hh ? (od ? O_TB + 32 : O_TA + 30) : (od ? O_TF + 32 : O_TE + 30)) + (j >> 1) * 2;
    u32 tB = hh ? kb + (od ? O_TD : O_TC) + 32 + (j >> 1) * 2 : tA + 64;
    constexpr u64 SAT = (u64)(O_TA + 30) | ((u64)(O_TE + 30) << 12) | ((u64)(O_TA + 94) << 24) | ((u64)(O_TC + 96) << 36) | ((u64)(O_TE + 158) << 48);
    u32 sa = kb + (lane < 20 ? (lane & 3) * (CS + 2) + (u32)((SAT >> (12 * (wh < 5 ? wh : 0))) & 0xfff) : O_Y);
    u32 zA = kb + (lane < 52 ? O_TA + lane * 16 : 1952);
    u32 cA = kb + 16 * lane, pA = lane ? cA - 8 : cA, cA2 = kb + (lane < 6 ? 1024 + 16 * lane : 1952);
    u32 m1 = lane == 32 ? 0xffffffffu : 0u, m0 = hh ? 0u : 0xffffffffu;
    u32 bn = kb + CS * (wh & 3) + 56 - 16 * (lane >> 4) + 8 * (lane & 3), bt = kb + CS * (wh & 3) - 8 * (lane >> 4) + 8 * (lane & 3);
    u32 ta = kb + lane * 4, fa = kb + j * TROW + hh * 16;
    asm volatile("" : "+v"(CR.bands[0]), "+v"(CR.bands[1]), "+v"(CR.bands[2]), "+v"(CR.bands[3]), "+v"(CR.bands[4]), "+v"(a0), "+v"(a1), "+v"(tA), "+v"(tB),
                 "+v"(sa), "+v"(zA), "+v"(cA), "+v"(pA), "+v"(cA2), "+v"(m1), "+v"(m0), "+v"(bn), "+v"(bt), "+v"(ta), "+v"(fa));
    STAMP(7);
    if (wid >= 2) {
      __builtin_amdgcn_s_sleep(SLP_W);
      const u32 row = mm * 16 + (lane >> 2);
      const u32 vo = row * 3584 + ((xc * 448) & ~127) + (lane & 3) * 128;
      dma4nt(vo, wid == 2 ? wg : wu, M_DUM);
    } else {
      u2v tg; u32 spins = 0;
      do { tg = lds_ldv<u2v>(M_TAG); } while (tg.y == 0 && ++spins < SPIN_CAP);
      STAMP(8);
      asm volatile("s_waitcnt vmcnt(7)" : "+v"(xt0), "+v"(xt1), "+v"(xt2), "+v"(xt3), "+v"(xt4), "+v"(xt5), "+v"(xt6), "+v"(xt7) :: "memory");
      STAMP(9);
      const u2v xt[8] = {xt0, xt1, xt2, xt3, xt4, xt5, xt6, xt7};
      u32 c = tg.x;
#pragma unroll
      for (int i = 0; i < 8; i++) c += xt[i].x + xt[i].y;
      const __amdgpu_buffer_rsrc_t rs = __builtin_amdgcn_make_buffer_rsrc(ws + W_XB + (R * 8 + S) * XBS, 0, XBS, 0x00020000);
      const u32 o = (8 * wid + (lane >> 3)) * 512 + (lane & 7) * 16;
#pragma unroll
      for (int i = 0; i < 4; i++)
        __builtin_amdgcn_raw_buffer_store_b128((u4v){xt[2 * i].x, xt[2 * i].y, xt[2 * i + 1].x, xt[2 * i + 1].y}, rs, o + i * 128, 0, 16);
      __builtin_amdgcn_raw_buffer_store_b32(c, rs, 8192 + wid * 256 + lane * 4, 0, 16);
    }
    asm volatile("s_cmp_lt_u32 %7, 2\n\ts_cbranch_scc1 1f\n\ts_waitcnt vmcnt(3)\n1:\n\ts_waitcnt vmcnt(5)"
                 : "+v"(p_ar), "+v"(p_ai), "+v"(p_br), "+v"(p_bi), "+v"(p_cr), "+v"(p_ci), "+v"(p_dt) : "s"(wid) : "memory", "scc");
    STAMP(1);

    const float dt = rbf(__builtin_amdgcn_exp2f(bl(p_dt) * 1.44269504f));
    f16v kd = {};
#pragma unroll 1
    for (int h = 0; h < 2; h++) {
      const u32 sh = 16 - 16 * h;
      const float a2 = dt * bh(p_ar << sh) * 1.44269504f, rv = dt * bh(p_ai << sh) * 0.159154943f;
      const f2 Bc = {dt * bh(p_br << sh), dt * bh(p_bi << sh)}, Cc = {bh(p_cr << sh), bh(p_ci << sh)};
      const f2 G = cmul(Cc, Bc, rot(Bc));
      const f2 w1 = cexp(a2, rv, 1.f), w256 = cexp(a2, rv, 256.f);
      f2 v8 = cexp(a2, -rv, 8.f);
      const f2 w1r = rot(w1), w256r = rot(w256);
      const f2 w2 = cmul(w1, w1, w1r), w2r = rot(w2), w4 = cmul(w2, w2, w2r), w4r = rot(w4);
      const f2 v16 = cmul(v8, v8, rot(v8)), v16r = rot(v16);
#define TW(r, v) lds_st<u32>(ta + (r) * TROW, pk2((v).x, (v).y))
      f2 fz = G, ge = v16, go = v8, fh = cmul(G, w256, w256r);
      TW(0, fz); TW(21, ((f2){1.f, 0.f})); TW(22, go); TW(23, ge); TW(8, fh);
      f2 t2 = cmul(fh, w2, w2r); TW(9, t2); t2 = cmul(t2, w2, w2r); TW(10, t2); t2 = cmul(t2, w2, w2r); TW(11, t2);
#pragma unroll
      for (int i = 1; i < 16; i++) {
        go = cmul(go, v16, v16r); TW(22 + 2 * i, go);
        if (i < 15) { ge = cmul(ge, v16, v16r); TW(23 + 2 * i, ge); }
        if (i < 8) { fz = cmul(fz, w1, w1r); TW(i, fz); }
        else {
          fh = cmul(fh, w256, w256r);
          if (i < 10) { TW(12 + (i - 8), fh); f2 t4 = cmul(fh, w4, w4r); TW(14 + (i - 8), t4); }
          else if (i < 15) TW(16 + (i - 10), fh);
        }
      }
      bf8 tf[16];
#pragma unroll
      for (int s = 0; s < 8; s++) { tf[2 * s] = lds_ld<bf8>(fa + 32 * s); tf[2 * s + 1] = lds_ld<bf8>(fa + 21 * TROW + 32 * s); }
      __builtin_amdgcn_sched_barrier(0);
#pragma unroll
      for (int s = 0; s < 8; s++) kd = __builtin_amdgcn_mfma_f32_32x32x16_bf16(tf[2 * s], tf[2 * s + 1], kd, 0, 0, 0);
    }
#pragma unroll
    for (int q = 0; q < 4; q++) lds_st<u4v>(zA + q * CS, (u4v){0, 0, 0, 0});
    STAMP(2);
    {
      lds_st<u2v>(a0, (u2v){pk2(kd[0], kd[1]), pk2(kd[2], kd[3])});
      const float en = DPPF(kd[4], 0x130, 0xf);
      const u32 e00 = pk2(kd[4], kd[4]);
      lds_st<u4v>(a1, (u4v){e00 & (m0 | m1), (pk2(kd[5], kd[6]) & m0) | (e00 & m1 & 0xffffu), pk2(kd[6], kd[6]) & m0, pk2(kd[7], en) & m0});
      const u4v c = lds_ld<u4v>(cA), c2 = lds_ld<u4v>(cA2);
      const u2v p = lds_ld<u2v>(pA), p2 = lds_ld<u2v>(cA2 - 8);
      const u32 v01 = hh ? pk2(kd[4], kd[5]) : pk2(kd[8], kd[9]), v23 = hh ? pk2(kd[6], kd[7]) : pk2(kd[10], kd[11]);
#pragma unroll
      for (int q = 0; q < 4; q++) {
        lds_st<u16>(tA + q * (CS + 2), (u16)v01); lds_st<u16>(tA + q * (CS + 2) + 32, (u16)(v01 >> 16));
        lds_st<u16>(tB + q * (CS + 2), (u16)v23); lds_st<u16>(tB + q * (CS + 2) + 32, (u16)(v23 >> 16));
      }
      const u32 k8 = pk2(kd[8], kd[8]);
      const u32 al = rl(k8, 0), k2048 = rl(k8, 32);
      lds_st<u16>(sa, (u16)(lane < 8 ? 0u : lane < 16 ? al : k2048));
      lds_st<u32>(M_AL + wid * 4, al << 16);
#define SHIFTS(c, p, A) do { \
      const u32 s0 = __builtin_amdgcn_alignbit(p.y, p.x, 16), s1 = __builtin_amdgcn_alignbit(c.x, p.y, 16), s2 = __builtin_amdgcn_alignbit(c.y, c.x, 16), \
                s3 = __builtin_amdgcn_alignbit(c.z, c.y, 16), s4 = __builtin_amdgcn_alignbit(c.w, c.z, 16); \
      lds_st<u4v>(A + CS, (u4v){s1, s2, s3, s4}); lds_st<u4v>(A + 2 * CS, (u4v){p.y, c.x, c.y, c.z}); lds_st<u4v>(A + 3 * CS, (u4v){s0, s1, s2, s3}); } while (0)
      SHIFTS(c, p, cA);
      SHIFTS(c2, p2, cA2);
    }
    MEMF();
#pragma unroll
    for (int a = 2; a <= 4; a++) {
      CR.T[2 * (a - 2)] = cat(trd(bt + O_TA + 32 * a - 32), trd(bt + O_TB + 32 * a - 32));
      CR.T[2 * (a - 2) + 1] = cat(trd(bt + O_TC + 32 * a - 32), trd(bt + O_TD + 32 * a - 32));
    }
#pragma unroll
    for (int a = 4; a <= 7; a++) CR.T[6 + a - 4] = cat(trd(bt + O_TE + 32 * a - 96), trd(bt + O_TF + 32 * a - 96));
#pragma unroll
    for (int m = 0; m < 17; m++) CR.An[m] = cat(trd(bn + 64 * m + 8), trd(bn + 64 * m));
    u32 ub = UB + wid * UCH + 32 + 32 * (lane & 15) + 16 * (lane >> 4);
    u32 ya = kb + O_Y + 64 * (lane & 15) + 16 * (lane >> 4), fl = M_YF + wid * 4;
    asm volatile("" : "+v"(ub), "+v"(ya), "+v"(fl));
    STAMP(3);
    asm volatile("s_waitcnt lgkmcnt(0)\n\ts_barrier" ::: "memory");
    STAMP(4);
    if (hf == 0) conv<0>(ub, ya, fl, CR); else conv<1>(ub, ya, fl, CR);
    STAMP(5);
    asm volatile("s_waitcnt vmcnt(0) lgkmcnt(0)" ::: "memory");
    STAMP_FLUSH();
    return;
  }

  const int cw = wid - 4;
  const int sp = xc * 32 + mm;
  u32 ep = 0;
  if (cw == 0) ep = __hip_atomic_load((GAS u32*)(ws + W_EP), __ATOMIC_RELAXED, __HIP_MEMORY_SCOPE_AGENT);
  const u4v xr0 = __builtin_nontemporal_load((const GAS u4v*)(x + (size_t)(8 * sp + 2 * cw) * 512) + lane);
  const u4v xr1 = __builtin_nontemporal_load((const GAS u4v*)(x + (size_t)(8 * sp + 2 * cw + 1) * 512) + lane);
  const u4v wv = ((const GAS u4v*)wr)[lane];
  const u2v dk = *(const GAS u2v*)(dsk + 4 * cg);
  if (cw == 3) lds_st<u32>(MB + lane * 4, 0u);
  asm volatile("s_waitcnt lgkmcnt(0)\n\ts_barrier" ::: "memory");
  u32 tag;
  if (cw == 0) {
    tag = __builtin_amdgcn_readfirstlane(ep) + 1u;
    if (lane == 0) lds_stv<u2v>(M_TAG, (u2v){tag, 1u});
  }
  {
    const bf2 ones = __builtin_bit_cast(bf2, 0x3F803F80u);
    float s0 = 0, q0 = 0, d0 = 0, s1 = 0, q1 = 0, d1 = 0, sw = 0;
#pragma unroll
    for (int i = 0; i < 4; i++) {
      const bf2 a = asbf2(xr0[i]), b = asbf2(xr1[i]), c = asbf2(wv[i]);
      s0 = __builtin_amdgcn_fdot2_f32_bf16(a, ones, s0, false); q0 = __builtin_amdgcn_fdot2_f32_bf16(a, a, q0, false); d0 = __builtin_amdgcn_fdot2_f32_bf16(a, c, d0, false);
      s1 = __builtin_amdgcn_fdot2_f32_bf16(b, ones, s1, false); q1 = __builtin_amdgcn_fdot2_f32_bf16(b, b, q1, false); d1 = __builtin_amdgcn_fdot2_f32_bf16(b, c, d1, false);
      sw = __builtin_amdgcn_fdot2_f32_bf16(c, ones, sw, false);
    }
    auto sw32 = [](float a, float b) { auto r = __builtin_amdgcn_permlane32_swap(__float_as_uint(a), __float_as_uint(b), false, false); return __uint_as_float(r[0]) + __uint_as_float(r[1]); };
    float s = sw32(s0, s1), q = sw32(q0, q1), dd = sw32(d0, d1); sw = sw32(sw, sw);
#define RED(v) v += DPPF(v, 0x128, 0xf);
    s += DPPF(s, 0x128, 0xf); q += DPPF(q, 0x128, 0xf); dd += DPPF(dd, 0x128, 0xf); sw += DPPF(sw, 0x128, 0xf);
    s += DPPF(s, 0x124, 0xf); q += DPPF(q, 0x124, 0xf); dd += DPPF(dd, 0x124, 0xf); sw += DPPF(sw, 0x124, 0xf);
    s += DPPF(s, 0x122, 0xf); q += DPPF(q, 0x122, 0xf); dd += DPPF(dd, 0x122, 0xf); sw += DPPF(sw, 0x122, 0xf);
    s += DPPF(s, 0x121, 0xf); q += DPPF(q, 0x121, 0xf); dd += DPPF(dd, 0x121, 0xf); sw += DPPF(sw, 0x121, 0xf);
    s += DPPF(s, 0x142, 0xa); q += DPPF(q, 0x142, 0xa); dd += DPPF(dd, 0x142, 0xa); sw += DPPF(sw, 0x142, 0xa);
    const float mean = s * (1.f / 512.f);
    const float var = fmaxf(q * (1.f / 512.f) - mean * mean, 0.f);
    const float mb = rbf(mean), sb = rbf(__builtin_amdgcn_sqrtf(rbf(rbf(var) + 1e-5f)));
    const float r = (dd - mb * sw) * __builtin_amdgcn_rcpf(sb);
    const u32 ms = pk2(mb, sb), rb = pk2(r, r) & 0xffffu;
    const u32 ms0 = rl(ms, 31), ms1 = rl(ms, 63), rr = rl(rb, 31) | (rl(rb, 63) << 16);
    if (cw != 0) {
      u2v tg; u32 spins = 0;
      do { tg = lds_ldv<u2v>(M_TAG); } while (tg.y == 0 && ++spins < SPIN_CAP);
      tag = __builtin_amdgcn_readfirstlane(tg.x);
    }
    const __amdgpu_buffer_rsrc_t rs = __builtin_amdgcn_make_buffer_rsrc(ws + W_ST, 0, 16384, 0x00020000);
    if (lane == 0) __builtin_amdgcn_raw_buffer_store_b128((u4v){ms0, ms1, rr, ms0 + ms1 + rr + tag + tag - 1u}, rs, cw * 4096 + sp * 16, 0, 16);
  }
  STAMP(1);
  const u32 chs = tag + tag - 1u;
  u4v su0, su1, su2, su3, xu0, xu1, xu2, xu3; u32 xk;
  const u32 so = W_ST + (64 * cw + lane) * 16;
  const u32 xb = W_XB + ((8 * cw + (lane >> 3)) * 8 + xc) * XBS;
  const u32 xo = xb + (mm >> 1) * 512 + (lane & 7) * 16, xko = xb + 8192 + (mm >> 1) * 32 + (lane & 7) * 4;
  const char* ws1 = ws + 4096; const char* ws2 = ws + 8192; const char* ws3 = ws + 12288;
#define LOAD_S() do { GLD4_SC1(su0, so, ws, 0); GLD4_SC1(su1, so, ws1, 0); GLD4_SC1(su2, so, ws2, 0); GLD4_SC1(su3, so, ws3, 0); } while (0)
#define LOAD_X() do { GLD4_SC1(xu0, xo, ws, 0); GLD4_SC1(xu1, xo, ws, 128); GLD4_SC1(xu2, xo, ws, 256); GLD4_SC1(xu3, xo, ws, 384); GLD1_SC1(xk, xko, ws, 0); } while (0)
#define OK_S() ((((su0.x + su0.y + su0.z + chs) ^ su0.w) | ((su1.x + su1.y + su1.z + chs) ^ su1.w) | ((su2.x + su2.y + su2.z + chs) ^ su2.w) | ((su3.x + su3.y + su3.z + chs) ^ su3.w)) == 0)
#define SUM4(v) (v.x + v.y + v.z + v.w)
#define OK_X() (SUM4(xu0) + SUM4(xu1) + SUM4(xu2) + SUM4(xu3) + tag == xk)
  __builtin_amdgcn_s_sleep(SLP_S);
  LOAD_S();
  __builtin_amdgcn_s_sleep(SLP_X);
  LOAD_X();
  const u32 fb = FB + cw * 12288;
  const int fp = xc * 14 + mm;
#define PULL() do { \
    const u32 go = (128 * cw + (lane >> 1)) * 3584 + fp * 32 + (lane & 1) * 16; \
    constexpr u32 GS = 32 * 3584 - 1024; \
    dma16x4(go, go + GS, go + 2 * GS, go + 3 * GS, wg, fb); \
    dma16x4(go, go + GS, go + 2 * GS, go + 3 * GS, wu, fb + 4096); \
    const u32 dof = (16 * fp + (lane >> 4)) * 1024 + 256 * cw + (lane & 15) * 16; \
    dma16x4(dof, dof + 3072, dof + 6144, dof + 9216, wd, fb + 8192); } while (0)
  asm volatile("s_waitcnt vmcnt(5)" : "+v"(su0), "+v"(su1), "+v"(su2), "+v"(su3) :: "memory");
  STAMP(2);
  for (u32 spins = 0; !__all(OK_S()) && spins < SPIN_CAP; spins++) {
    asm volatile("s_sleep 2\n\tglobal_load_dwordx4 %0, %4, %5 sc1\n\tglobal_load_dwordx4 %1, %4, %6 sc1\n\tglobal_load_dwordx4 %2, %4, %7 sc1\n\t"
                 "global_load_dwordx4 %3, %4, %8 sc1\n\ts_waitcnt vmcnt(0)"
                 : "=&v"(su0), "=&v"(su1), "=&v"(su2), "=&v"(su3) : "v"(so), "s"(ws), "s"(ws1), "s"(ws2), "s"(ws3) : "memory");
  }
  STAMP(3);
  u32 k1, k2;
  {
    const u32 ib = 0xffffu - (512 * cw + 8 * lane);
    auto key = [&](u32 b, int i) { u32 o = b ^ ((u32)((int)b >> 31) | 0x80000000u); return (o & 0xffff0000u) | (ib - i); };
    k1 = key(su0.z << 16, 0); k2 = 0;
    auto ins = [&](u32 k) { k2 = umax(k2, umin(k1, k)); k1 = umax(k1, k); };
    ins(key(su0.z & 0xffff0000u, 1)); ins(key(su1.z << 16, 2)); ins(key(su1.z & 0xffff0000u, 3)); ins(key(su2.z << 16, 4));
    ins(key(su2.z & 0xffff0000u, 5)); ins(key(su3.z << 16, 6)); ins(key(su3.z & 0xffff0000u, 7));
    const u32 K1 = wave_umax(k1), K2 = wave_umax(k1 == K1 ? k2 : k1);
    if (lane == 0) lds_stv<u2v>(M_TOP + cw * 8, (u2v){K1, K2});
    u4v ta, tb; u32 spins = 0;
    do { ta = lds_ldv<u4v>(M_TOP); tb = lds_ldv<u4v>(M_TOP + 16); } while ((ta.x == 0 || ta.z == 0 || tb.x == 0 || tb.z == 0) && ++spins < SPIN_CAP);
    const u32 a1 = umax(ta.x, ta.z), a2 = umax(umin(ta.x, ta.z), umax(ta.y, ta.w)), b1 = umax(tb.x, tb.z), b2 = umax(umin(tb.x, tb.z), umax(tb.y, tb.w));
    k1 = __builtin_amdgcn_readfirstlane(umax(a1, b1)); k2 = __builtin_amdgcn_readfirstlane(umax(umin(a1, b1), umax(a2, b2)));
  }
  const int t0 = (0xffffu - (k1 & 0xffffu)) & 2047, t1 = (0xffffu - (k2 & 0xffffu)) & 2047;
  u4v xs0, xs1, xs2, xs3;
  if (prod) {
    const u32 xso = ((lane & 1) ? t1 : t0) * 1024 + 256 * cw + 16 * (lane >> 4);
    GLD4(xs0, xso, x, 0); GLD4(xs1, xso, x, 64); GLD4(xs2, xso, x, 128); GLD4(xs3, xso, x, 192);
  }
  if (w == 0 && cw == 0 && lane == 0) __hip_atomic_store((GAS u32*)(ws + W_EP), tag, __ATOMIC_RELAXED, __HIP_MEMORY_SCOPE_AGENT);
  if ((t0 >> 3) == 64 * cw + lane) { lds_st<u4v>(M_SEL, (u4v){su0.x, su0.y, su1.x, su1.y}); lds_st<u4v>(M_SEL + 16, (u4v){su2.x, su2.y, su3.x, su3.y}); }
  if ((t1 >> 3) == 64 * cw + lane) { lds_st<u4v>(M_SEL + 32, (u4v){su0.x, su0.y, su1.x, su1.y}); lds_st<u4v>(M_SEL + 48, (u4v){su2.x, su2.y, su3.x, su3.y}); }
  float na[8], nb[8];
  {
    const u32 msv[8] = {su0.x, su0.y, su1.x, su1.y, su2.x, su2.y, su3.x, su3.y};
#pragma unroll
    for (int i = 0; i < 8; i++) { na[i] = __builtin_amdgcn_rcpf(bh(msv[i])); nb[i] = -bl(msv[i]) * na[i]; }
  }
  asm volatile("" : "+v"(na[0]), "+v"(na[1]), "+v"(na[2]), "+v"(na[3]), "+v"(na[4]), "+v"(na[5]), "+v"(na[6]), "+v"(na[7]),
               "+v"(nb[0]), "+v"(nb[1]), "+v"(nb[2]), "+v"(nb[3]), "+v"(nb[4]), "+v"(nb[5]), "+v"(nb[6]), "+v"(nb[7]));
  STAMP(4);
#define XW "4"
  asm volatile("s_cmp_lt_u32 %5, 111\n\ts_cbranch_scc1 1f\n\ts_waitcnt vmcnt(0)\n1:\n\ts_waitcnt vmcnt(" XW ")"
               : "+v"(xu0), "+v"(xu1), "+v"(xu2), "+v"(xu3), "+v"(xk) : "s"(w - 1) : "memory", "scc");
  for (u32 spins = 0; !__all(OK_X()) && spins < SPIN_CAP; spins++) {
    asm volatile("s_sleep 1\n\tglobal_load_dwordx4 %0, %5, %7 sc1\n\tglobal_load_dwordx4 %1, %5, %7 offset:128 sc1\n\tglobal_load_dwordx4 %2, %5, %7 offset:256 sc1\n\t"
                 "global_load_dwordx4 %3, %5, %7 offset:384 sc1\n\tglobal_load_dword %4, %6, %7 sc1\n\ts_waitcnt vmcnt(0)"
                 : "=&v"(xu0), "=&v"(xu1), "=&v"(xu2), "=&v"(xu3), "=&v"(xk) : "v"(xo), "v"(xko), "s"(ws) : "memory");
  }
  STAMP(5);
  u32 xa[8] = {xu0.x, xu0.z, xu1.x, xu1.z, xu2.x, xu2.z, xu3.x, xu3.z};
  u32 xcv[8] = {xu0.y, xu0.w, xu1.y, xu1.w, xu2.y, xu2.w, xu3.y, xu3.w};
  {
    u4v c0, c1, c2, c3;
#pragma unroll
    for (int i = 0; i < 4; i++) {
      const int r0 = 2 * i, r1 = 2 * i + 1;
      const f2 p0 = __builtin_elementwise_fma((f2){bl(xa[r0]), bh(xa[r0])}, (f2){na[r0], na[r0]}, (f2){nb[r0], nb[r0]});
      const f2 p1 = __builtin_elementwise_fma((f2){bl(xa[r1]), bh(xa[r1])}, (f2){na[r1], na[r1]}, (f2){nb[r1], nb[r1]});
      const f2 p2 = __builtin_elementwise_fma((f2){bl(xcv[r0]), bh(xcv[r0])}, (f2){na[r0], na[r0]}, (f2){nb[r0], nb[r0]});
      const f2 p3 = __builtin_elementwise_fma((f2){bl(xcv[r1]), bh(xcv[r1])}, (f2){na[r1], na[r1]}, (f2){nb[r1], nb[r1]});
      c0[i] = pk2(p0.x, p1.x); c1[i] = pk2(p0.y, p1.y); c2[i] = pk2(p2.x, p3.x); c3[i] = pk2(p2.y, p3.y);
    }
    const u32 ua = UB + 512 + (64 * cw + lane) * 16;
    lds_st<u4v>(ua, c0); lds_st<u4v>(ua + UCH, c1); lds_st<u4v>(ua + 2 * UCH, c2); lds_st<u4v>(ua + 3 * UCH, c3);
  }
  asm volatile("s_waitcnt lgkmcnt(0)\n\ts_barrier" ::: "memory");
  STAMP(6);

  if (prod) {
    PULL();
    const u32 msel = lds_ld<u32>(M_SEL + (lane & 1) * 32 + (((lane & 1) ? t1 : t0) & 7) * 4);
    const float a = __builtin_amdgcn_rcpf(bh(msel)), b = -bl(msel) * a;
    asm volatile("s_waitcnt vmcnt(0)" : "+v"(xs0), "+v"(xs1), "+v"(xs2), "+v"(xs3) :: "memory");
    STAMP(7);
    auto nrm = [&](u4v v) { u4v r;
#pragma unroll
      for (int i = 0; i < 4; i++) r[i] = pk2(fmaf(bl(v[i]), a, b), fmaf(bh(v[i]), a, b));
      return __builtin_bit_cast(bf8, r); };
    const bf8 xf[4] = {nrm(xs0), nrm(xs1), nrm(xs2), nrm(xs3)};
    const int g = lane >> 4, q = (lane >> 2) & 3, p = lane & 3;
    const u32 ga = fb + (8 * g + q) * 32 + 8 * p;
    f4 ag = {0, 0, 0, 0}, au = {0, 0, 0, 0};
#pragma unroll
    for (int s = 0; s < 4; s++) {
      ag = mma(cat(trd(ga + s * 1024), trd(ga + s * 1024 + 128)), xf[s], ag);
      au = mma(cat(trd(ga + 4096 + s * 1024), trd(ga + 4096 + s * 1024 + 128)), xf[s], au);
    }
    if ((lane & 15) < 2) {
      lds_st<f4>(M_PART + cw * 256 + (lane & 1) * 64 + g * 16, ag);
      lds_st<f4>(M_PART + cw * 256 + 128 + (lane & 1) * 64 + g * 16, au);
    }
    MEMF();
    if (lane == 0) lds_stv<u32>(M_FF + cw * 4, 1u);
    u4v ff; u32 spins = 0;
    do { ff = lds_ldv<u4v>(M_FF); } while ((ff.x & ff.y & ff.z & ff.w) == 0 && ++spins < SPIN_CAP);
    MEMF();
    f4 sg = {0, 0, 0, 0}, su = {0, 0, 0, 0};
#pragma unroll
    for (int v = 0; v < 4; v++) {
      sg += lds_ld<f4>(M_PART + v * 256 + (lane & 1) * 64 + g * 16);
      su += lds_ld<f4>(M_PART + v * 256 + 128 + (lane & 1) * 64 + g * 16);
    }
    float hv[4];
#pragma unroll
    for (int i = 0; i < 4; i++) { const float gg = rbf(sg[i]); hv[i] = rbf(gg * sigm(gg)) * rbf(su[i]); }
    const s4v hb = __builtin_bit_cast(s4v, ((u2v){pk2(hv[0], hv[1]), pk2(hv[2], hv[3])}));
    const u32 da = fb + 8192 + g * 1024 + q * 256 + 8 * p;
    const __amdgpu_buffer_rsrc_t rs = __builtin_amdgcn_make_buffer_rsrc(ws + W_FB + fp * 8192, 0, 8192, 0x00020000);
    const u32 fo = (32 * cw + g) * 64 + (lane & 1) * 32 + (lane & 2) * 8;
#pragma unroll
    for (int t = 0; t < 8; t++) {
      const f4 y = __builtin_amdgcn_mfma_f32_16x16x16bf16_1k(__builtin_bit_cast(s4v, trd(da + 32 * t)), hb, (f4){0, 0, 0, 0}, 0, 0, 0);
      const u4v o = {__float_as_uint((lane & 2) ? y[2] : y[0]), tag, __float_as_uint((lane & 2) ? y[3] : y[1]), tag};
      if ((lane & 12) == 0) __builtin_amdgcn_raw_buffer_store_b128(o, rs, fo + t * 256, 0, 16);
    }
    STAMP(8);
  }

  const int up = (cw & 1) ^ hf;
  const int tile = 2 * cw + up;
  const int tl = 8 * (lane & 31) + 4 * (lane >> 5), tr0 = 256 * tile + tl;
  float xv[4][4], uv[4][4], pre[4][4], gt[4][4];
#pragma unroll
  for (int r = 0; r < 4; r++) {
    auto s0 = __builtin_amdgcn_permlane32_swap(xa[r], xa[r + 4], false, false);
    auto s1 = __builtin_amdgcn_permlane32_swap(xcv[r], xcv[r + 4], false, false);
    const u32 e0 = up ? s0[1] : s0[0], e1 = up ? s1[1] : s1[0];
    xv[0][r] = bl(e0); xv[1][r] = bh(e0); xv[2][r] = bl(e1); xv[3][r] = bh(e1);
  }
  {
    const f4 alv = lds_ld<f4>(M_AL);
    const float dv[4] = {bl(dk.x), bh(dk.x), bl(dk.y), bh(dk.y)};
    const u32 ua = UB + 512 + tr0 * 2;
    const u32 ca = tile >= 4 ? ua - 2064 : UB;
#pragma unroll
    for (int c = 0; c < 4; c++) {
      const u2v a = lds_ld<u2v>(ua + c * UCH), p = lds_ld<u2v>(ca + c * UCH), n = lds_ld<u2v>(ca + c * UCH + 8);
      uv[c][0] = bl(a.x); uv[c][1] = bh(a.x); uv[c][2] = bl(a.y); uv[c][3] = bh(a.y);
      pre[c][0] = uv[c][0] * dv[c] - alv[c] * bh(p.y); pre[c][1] = uv[c][1] * dv[c] - alv[c] * bl(n.x);
      pre[c][2] = uv[c][2] * dv[c] - alv[c] * bh(n.x); pre[c][3] = uv[c][3] * dv[c] - alv[c] * bl(n.y);
#pragma unroll
      for (int r = 0; r < 4; r++) gt[c][r] = uv[c][r] * sigm(uv[c][r]);
    }
  }
  STAMP(9);
  {
    u4v yf; u32 spins = 0;
    do { __builtin_amdgcn_s_sleep(1); yf = lds_ldv<u4v>(M_YF + cw * 16); } while (umin(umin(yf.x, yf.y), umin(yf.z, yf.w)) == 0 && ++spins < SPIN_CAP);
  }
  MEMF();
  STAMP(10);
#pragma unroll
  for (int c = 0; c < 4; c++) {
    const f4 y = lds_ld<f4>(c * KREG + O_Y + cw * 1024 + tl * 4);
#pragma unroll
    for (int r = 0; r < 4; r++) pre[c][r] += y[r];
  }
  const bool late = (tr0 >> 2) == (t0 >> 2) || (tr0 >> 2) == (t1 >> 2);
  char* op = (char*)out + (size_t)tr0 * 1024 + cg * 8;
  if (!late) {
#pragma unroll
    for (int r = 0; r < 4; r++)
      *(GAS u2v*)(op + r * 1024) = (u2v){pk2(fmaf(pre[0][r], gt[0][r], xv[0][r]), fmaf(pre[1][r], gt[1][r], xv[1][r])),
                                         pk2(fmaf(pre[2][r], gt[2][r], xv[2][r]), fmaf(pre[3][r], gt[3][r], xv[3][r]))};
  }
  STAMP(11);
  if ((t0 >> 8) == tile || (t1 >> 8) == tile) {
#pragma unroll 1
    for (int k = 0; k < 2; k++) {
      const int tk = k ? t1 : t0; const u32 kk = k ? k2 : k1;
      if ((tk >> 8) != tile) continue;
      const u32 fo = W_FB + (lane >> 1) * 8192 + cg * 64 + k * 32 + (lane & 1) * 16;
      const u32 fo3 = lane < 32 ? fo + 96 * 8192 : fo;
      u4v g0, g1, g2, g3;
      for (u32 spins = 0; spins < SPIN_CAP; spins++) {
        asm volatile("global_load_dwordx4 %0, %4, %8 sc1\n\tglobal_load_dwordx4 %1, %5, %8 sc1\n\tglobal_load_dwordx4 %2, %6, %8 sc1\n\t"
                     "global_load_dwordx4 %3, %7, %8 sc1\n\ts_waitcnt vmcnt(0)"
                     : "=&v"(g0), "=&v"(g1), "=&v"(g2), "=&v"(g3) : "v"(fo), "v"(fo + 32 * 8192), "v"(fo + 64 * 8192), "v"(fo3), "s"(ws) : "memory");
        if (__all(((g0.y ^ tag) | (g0.w ^ tag) | (g1.y ^ tag) | (g1.w ^ tag) | (g2.y ^ tag) | (g2.w ^ tag) | (g3.y ^ tag) | (g3.w ^ tag)) == 0)) break;
        __builtin_amdgcn_s_sleep(1);
      }
      float v0 = __uint_as_float(g0.x) + __uint_as_float(g1.x) + __uint_as_float(g2.x) + (lane < 32 ? __uint_as_float(g3.x) : 0.f);
      float v1 = __uint_as_float(g0.z) + __uint_as_float(g1.z) + __uint_as_float(g2.z) + (lane < 32 ? __uint_as_float(g3.z) : 0.f);
      v0 += DPPF(v0, 0x128, 0xf); v1 += DPPF(v1, 0x128, 0xf); v0 += DPPF(v0, 0x124, 0xf); v1 += DPPF(v1, 0x124, 0xf);
      v0 += DPPF(v0, 0x122, 0xf); v1 += DPPF(v1, 0x122, 0xf);
      float yv[4];
      {
        const u32 a = __float_as_uint(v0), b = __float_as_uint(v1);
        yv[0] = (__uint_as_float(rl(a, 0)) + __uint_as_float(rl(a, 16))) + (__uint_as_float(rl(a, 32)) + __uint_as_float(rl(a, 48)));
        yv[1] = (__uint_as_float(rl(b, 0)) + __uint_as_float(rl(b, 16))) + (__uint_as_float(rl(b, 32)) + __uint_as_float(rl(b, 48)));
        yv[2] = (__uint_as_float(rl(a, 1)) + __uint_as_float(rl(a, 17))) + (__uint_as_float(rl(a, 33)) + __uint_as_float(rl(a, 49)));
        yv[3] = (__uint_as_float(rl(b, 1)) + __uint_as_float(rl(b, 17))) + (__uint_as_float(rl(b, 33)) + __uint_as_float(rl(b, 49)));
      }
      const u32 ob = kk & 0xffff0000u;
      const float rs = rbf(sigm(__uint_as_float((ob & 0x80000000u) ? (ob ^ 0x80000000u) : (~ob & 0xffff0000u))));
#pragma unroll
      for (int c = 0; c < 4; c++) {
        const float upd = rbf(rbf(yv[c]) * rs);
#pragma unroll
        for (int r = 0; r < 4; r++) {
          const float b = rbf(uv[c][r] + upd);
          gt[c][r] = (tr0 + r == tk) ? b * sigm(b) : gt[c][r];
        }
      }
    }
    if (late) {
#pragma unroll
      for (int r = 0; r < 4; r++)
        *(GAS u2v*)(op + r * 1024) = (u2v){pk2(fmaf(pre[0][r], gt[0][r], xv[0][r]), fmaf(pre[1][r], gt[1][r], xv[1][r])),
                                           pk2(fmaf(pre[2][r], gt[2][r], xv[2][r]), fmaf(pre[3][r], gt[3][r], xv[3][r]))};
    }
    STAMP(12);
  }
}

extern "C" void kernel_launch(void* const* d_in, const int* in_sizes, int n_in, void* d_out, int out_size,
                              void* d_ws, size_t ws_size, hipStream_t stream) {
  (void)hipFuncSetAttribute((const void*)s4_moe_kernel, hipFuncAttributeMaxDynamicSharedMemorySize, LDS_BYTES);
  s4_moe_kernel<<<256, 512, LDS_BYTES, stream>>>(
      (const u16*)d_in[0], (const u16*)d_in[1], (const u16*)d_in[2], (const u16*)d_in[3], (const u16*)d_in[4],
      (const u16*)d_in[5], (const u16*)d_in[6], (const u16*)d_in[7], (const u16*)d_in[8], (const u16*)d_in[10],
      (const u16*)d_in[11], (const u16*)d_in[12], (const u16*)d_in[13], (u16*)d_out, (char*)d_ws);
}
```

```cpp
#define PROBE 0
#define PW 200
#define PWAVE 4
#define PULL_LATE
#include <hip/hip_runtime.h>
#include <hip/hip_bf16.h>
#include <stdint.h>

typedef unsigned short u16;
typedef unsigned int u32;
typedef unsigned long long u64;
typedef float f2 __attribute__((ext_vector_type(2)));
typedef float f4 __attribute__((ext_vector_type(4)));
typedef float f16v __attribute__((ext_vector_type(16)));
typedef u32 u2v __attribute__((ext_vector_type(2)));
typedef u32 u4v __attribute__((ext_vector_type(4)));
typedef __bf16 bf2 __attribute__((ext_vector_type(2)));
typedef __bf16 bf4 __attribute__((ext_vector_type(4)));
typedef __bf16 bf8 __attribute__((ext_vector_type(8)));
typedef short s4v __attribute__((ext_vector_type(4)));
#define LAS __attribute__((address_space(3)))
#define GAS __attribute__((address_space(1)))
#define DEVI __device__ __forceinline__

#define SLP_S 10
#define SLP_X 4
#define SLP_W 20
#define SPIN_CAP (1u << 18)

constexpr int KREG = 14592;
constexpr int TROW = 272;
constexpr int CS = 2112;
constexpr int O_TA = 1120, O_TB = 1248, O_TC = 1376, O_TD = 1504, O_TE = 1632, O_TF = 1792;
constexpr int O_Y = 8448;
constexpr int UB = 4 * KREG;
constexpr int UCH = 4608;
constexpr int FB = UB + 4 * UCH;
constexpr int MB = FB + 49152;
constexpr int M_TAG = MB;
constexpr int M_TOP = MB + 16;
constexpr int M_YF = MB + 64;
constexpr int M_FF = MB + 128;
constexpr int M_AL = MB + 144;
constexpr int M_SEL = MB + 192;
constexpr int M_PART = MB + 256;
constexpr int M_DUM = MB + 1280;
constexpr int LDS_BYTES = MB + 1536;

constexpr u32 W_EP = 0;
constexpr u32 W_ST = 4096;
constexpr u32 W_XB = 65536;
constexpr u32 XBS = 8704;
constexpr u32 W_FB = 4u << 20;
constexpr u32 W_STAMP = 8u << 20;

#undef STAMPS
#define STAMP(i) do { if ((i) == 0) asm volatile("s_memrealtime %0\n\ts_waitcnt lgkmcnt(0)" : "=s"(pt0) :: "memory"); if ((i) == PROBE) asm volatile("s_memrealtime %0\n\ts_waitcnt lgkmcnt(0)" : "=s"(ptk) :: "memory"); } while (0)
#define STAMP_DECL u64 pt0 = 0, ptk = 0
#define STAMP_FLUSH() do { if (blockIdx.x == PW && wid == PWAVE) { u64 e_ = pt0 + 3000 + 10 * (ptk - pt0); while (__builtin_amdgcn_s_memrealtime() < e_) {} } } while (0)
#define CODE_LINES 64

DEVI float bl(u32 v) { return __uint_as_float(v << 16); }
DEVI float bh(u32 v) { return __uint_as_float(v & 0xffff0000u); }
DEVI u32 pk2(float a, float b) { f2 v = {a, b}; return __builtin_bit_cast(u32, __builtin_convertvector(v, bf2)); }
DEVI bf2 asbf2(u32 v) { return __builtin_bit_cast(bf2, v); }
DEVI float rbf(float a) { return bl(pk2(a, a)); }
DEVI f2 cmul(f2 x, f2 y, f2 yr) { return __builtin_elementwise_fma(x.yy, yr, x.xx * y); }
DEVI f2 rot(f2 y) { return (f2){-y.y, y.x}; }
DEVI f2 cexp(float a2, float rev, float l) {
  float e = __builtin_amdgcn_exp2f(a2 * l), p = __builtin_amdgcn_fractf(rev * l);
  return (f2){e * __builtin_amdgcn_cosf(p), e * __builtin_amdgcn_sinf(p)};
}
DEVI float sigm(float v) { return __builtin_amdgcn_rcpf(1.f + __builtin_amdgcn_exp2f(v * -1.44269504f)); }
template <class T> DEVI T lds_ld(u32 a) { return *(const LAS T*)(uintptr_t)a; }
template <class T> DEVI void lds_st(u32 a, T v) { *(LAS T*)(uintptr_t)a = v; }
template <class T> DEVI T lds_ldv(u32 a) { return *(const volatile LAS T*)(uintptr_t)a; }
template <class T> DEVI void lds_stv(u32 a, T v) { *(volatile LAS T*)(uintptr_t)a = v; }
DEVI bf4 trd(u32 a) { return __builtin_amdgcn_ds_read_tr16_b64_v4bf16((LAS bf4*)(uintptr_t)a); }
DEVI bf8 cat(bf4 a, bf4 b) { return __builtin_shufflevector(a, b, 0, 1, 2, 3, 4, 5, 6, 7); }
DEVI f4 mma(bf8 a, bf8 b, f4 c) { return __builtin_amdgcn_mfma_f32_16x16x32_bf16(a, b, c, 0, 0, 0); }
DEVI u32 rl(u32 v, int l) { return __builtin_amdgcn_readlane(v, l); }
#define DPPF(v, ctl, rm) __uint_as_float(__builtin_amdgcn_update_dpp(0u, __float_as_uint(v), ctl, rm, 0xf, true))
#define DPPU(v, ctl, rm) __builtin_amdgcn_update_dpp(0u, v, ctl, rm, 0xf, true)
DEVI u32 umax(u32 a, u32 b) { return a > b ? a : b; }
DEVI u32 umin(u32 a, u32 b) { return a < b ? a : b; }
DEVI u32 wave_umax(u32 v) {
  v = umax(v, DPPU(v, 0x128, 0xf)); v = umax(v, DPPU(v, 0x124, 0xf)); v = umax(v, DPPU(v, 0x122, 0xf)); v = umax(v, DPPU(v, 0x121, 0xf));
  v = umax(v, DPPU(v, 0x142, 0xa)); v = umax(v, DPPU(v, 0x143, 0xc));
  return rl(v, 63);
}
#define MEMF() asm volatile("" ::: "memory")

#define GLD4_SC1(d, vo, base, imm) asm volatile("global_load_dwordx4 %0, %1, %2 offset:%3 sc1" : "=v"(d) : "v"(vo), "s"(base), "i"(imm) : "memory")
#define GLD1_SC1(d, vo, base, imm) asm volatile("global_load_dword %0, %1, %2 offset:%3 sc1" : "=v"(d) : "v"(vo), "s"(base), "i"(imm) : "memory")
#define GLD2_NT(d, vo, base, imm) asm volatile("global_load_dwordx2 %0, %1, %2 offset:%3 nt" : "=v"(d) : "v"(vo), "s"(base), "i"(imm) : "memory")
#define GLD1_NT(d, vo, base) asm volatile("global_load_dword %0, %1, %2 nt" : "=v"(d) : "v"(vo), "s"(base) : "memory")
#define GLDH(d, vo, base) asm volatile("global_load_ushort %0, %1, %2" : "=v"(d) : "v"(vo), "s"(base) : "memory")
#define GLD4(d, vo, base, imm) asm volatile("global_load_dwordx4 %0, %1, %2 offset:%3" : "=v"(d) : "v"(vo), "s"(base), "i"(imm) : "memory")
DEVI void dma16(u32 vo, const void* base, u32 ldsdst) {
  u32 keep;
  asm volatile("s_mov_b32 %0, m0\n\ts_mov_b32 m0, %3\n\ts_nop 0\n\tglobal_load_lds_dwordx4 %1, %2\n\ts_mov_b32 m0, %0" : "=&s"(keep) : "v"(vo), "s"(base), "s"(ldsdst) : "memory");
}
DEVI void dma16x4(u32 v0, u32 v1, u32 v2, u32 v3, const void* base, u32 ldsdst) {
  u32 keep;
  asm volatile("s_mov_b32 %0, m0\n\ts_mov_b32 m0, %6\n\ts_nop 0\n\tglobal_load_lds_dwordx4 %1, %5\n\tglobal_load_lds_dwordx4 %2, %5 offset:1024\n\t"
               "global_load_lds_dwordx4 %3, %5 offset:2048\n\tglobal_load_lds_dwordx4 %4, %5 offset:3072\n\ts_mov_b32 m0, %0"
               : "=&s"(keep) : "v"(v0), "v"(v1), "v"(v2), "v"(v3), "s"(base), "s"(ldsdst) : "memory");
}
DEVI void dma4nt(u32 vo, const void* base, u32 ldsdst) {
  u32 keep;
  asm volatile("s_mov_b32 %0, m0\n\ts_mov_b32 m0, %3\n\ts_nop 0\n\tglobal_load_lds_dword %1, %2 nt\n\ts_mov_b32 m0, %0" : "=&s"(keep) : "v"(vo), "s"(base), "s"(ldsdst) : "memory");
}
DEVI void dma4(u32 vo, const void* base, u32 ldsdst) {
  u32 keep;
  asm volatile("s_mov_b32 %0, m0\n\ts_mov_b32 m0, %3\n\ts_nop 0\n\tglobal_load_lds_dword %1, %2\n\ts_mov_b32 m0, %0" : "=&s"(keep) : "v"(vo), "s"(base), "s"(ldsdst) : "memory");
}

DEVI bf8 band(int lane, int co, int hw) {
  int lo = (lane & 15) + co - hw - 8 * (lane >> 4);
  u32 m = (u32)(((u64)((1u << (2 * hw + 1)) - 1) << (lo + 24)) >> 24);
  u4v r;
#pragma unroll
  for (int i = 0; i < 4; i++) r[i] = ((m >> (2 * i)) & 1u) * 0x3F80u + ((m >> (2 * i + 1)) & 1u) * 0x3F800000u;
  return __builtin_bit_cast(bf8, r);
}

struct Sched {
  int n, nl, nd;
  int kind[128], p0[128], p1[128], ld[128], tile[128];
  int loff[96];
  int dsrc[40];
  int last[4];
  int lcum[129], ccum[129], yat[4];
};
constexpr int PFD = 8;
constexpr int lswap(int k, int n) { return (k ^ 1) < n ? (k ^ 1) : k; }
constexpr int tile_of(int hf, int i) { return hf == 0 ? (i == 0 ? 0 : i == 1 ? 3 : i == 2 ? 4 : 7) : (i == 0 ? 1 : i == 1 ? 2 : i == 2 ? 5 : 6); }
struct Sched2 { Sched s; int dmap[32]; };
constexpr Sched2 make_sched(int hf) {
  Sched2 r{}; Sched& s = r.s;
  for (int i = 0; i < 4; i++) {
    const int q = tile_of(hf, i), pq = i ? tile_of(hf, i - 1) : -1;
    for (int e = 0; e < 6; e++) {
      const bool n2 = (e <= q - 2) && (e >= q - 4) && !((e <= pq - 2) && (e >= pq - 4));
      const bool n3 = (e <= q - 4) && !(e <= pq - 4);
      if (!(n2 || n3)) continue;
      const int l24 = s.nl++, l16 = s.nl++;
      s.loff[l24] = 64 * (8 * e + 7) - 16; s.loff[l16] = 64 * (8 * e + 7);
      const int bsel[6] = {0, 0, 1, 2, 3, 4}, lsel[6] = {l24, l16, l16, l16, l24, l16};
      const int dm[6] = {4 * e, 4 * e + 1, 4 * e + 2, 4 * e + 3, 24 + 2 * e, 24 + 2 * e + 1};
      for (int k = 0; k < 6; k++) {
        if (k < 4 ? !n2 : !n3) continue;
        s.kind[s.n] = 1; s.p0[s.n] = bsel[k]; s.p1[s.n] = s.nd; s.ld[s.n] = lsel[k]; s.tile[s.n] = i;
        r.dmap[dm[k]] = s.nd; s.dsrc[s.nd++] = s.n; s.n++;
      }
    }
    for (int m = 0; m < 17; m++) {
      const int pp = 8 * q - m;
      if (pp < -7) continue;
      s.loff[s.nl] = 64 * (pp + 7);
      s.kind[s.n] = 0; s.p0[s.n] = m; s.ld[s.n] = s.nl++; s.tile[s.n] = i; s.n++;
    }
    for (int a = 2; a <= 4; a++)
      for (int h = 0; h < 2; h++)
        if (q - a >= 0) { s.kind[s.n] = 2; s.p0[s.n] = 2 * (q - a) + h; s.p1[s.n] = 2 * (a - 2) + h; s.ld[s.n] = -1; s.tile[s.n] = i; s.n++; }
    for (int a = 4; a <= 7; a++)
      if (q - a >= 0) { s.kind[s.n] = 2; s.p0[s.n] = 12 + (q - a); s.p1[s.n] = 6 + (a - 4); s.ld[s.n] = -1; s.tile[s.n] = i; s.n++; }
    s.last[i] = s.n - 1;
    s.yat[i] = s.n + 1;
  }
  int nl = PFD, nc = 0, mx = 0;
  s.lcum[0] = nl; s.ccum[0] = 0;
  for (int i = 0; i < s.n; i++) {
    if (s.ld[i] > mx) mx = s.ld[i];
    int slots = 2;
    if (nl < s.nl && nl <= (mx | 1) + PFD - 1) { nl++; slots--; }
    while (slots > 0 && nc < 2 * s.nd && s.dsrc[nc / 2] <= i - 2) { nc++; slots--; }
    if (slots > 0 && nl < s.nl && nl <= (mx | 1) + PFD - 1) { nl++; slots--; }
    s.lcum[i + 1] = nl; s.ccum[i + 1] = nc;
  }
  return r;
}
template <int HF> struct SchedOf { static constexpr Sched2 v = make_sched(HF); };

struct ConvRegs { bf8 An[17], T[10], bands[5]; };
template <int HF, int I>
DEVI void conv_step(u32 ub, u32 yaddr, u32 faddr, const ConvRegs& R, bf8 (&fr)[96], f4 (&D)[40], u32 (&P)[80], f4 (&acc)[4]) {
  constexpr const Sched2& S2 = SchedOf<HF>::v; constexpr const Sched& S = S2.s;
  if constexpr (I < S.n) {
    const f4 z = {0.f, 0.f, 0.f, 0.f};
    constexpr int t = S.tile[I], p0 = S.p0[I], p1 = S.p1[I], ld = S.ld[I];
    if constexpr (S.kind[I] == 0) acc[t] = mma(R.An[p0], fr[ld], p0 == 0 ? z : acc[t]);
    else if constexpr (S.kind[I] == 1) D[p1] = mma(fr[ld], R.bands[p0], z);
    else {
      constexpr int d0 = S2.dmap[2 * S.p0[I]], d1 = S2.dmap[2 * S.p0[I] + 1];
      static_assert(S.ccum[I] >= 2 * d0 + 2 && S.ccum[I] >= 2 * d1 + 2, "window not converted in time");
      acc[t] = mma(__builtin_bit_cast(bf8, ((u4v){P[2 * d0], P[2 * d0 + 1], P[2 * d1], P[2 * d1 + 1]})), R.T[p1], acc[t]);
    }
    __builtin_amdgcn_sched_barrier(0);
    constexpr int l0 = S.lcum[I], l1 = S.lcum[I + 1], c0 = S.ccum[I], c1 = S.ccum[I + 1];
    if constexpr (l0 < l1) { constexpr int k = lswap(l0, S.nl), o = S.loff[k]; fr[k] = lds_ld<bf8>(ub + o); }
    if constexpr (l0 + 1 < l1) { constexpr int k = lswap(l0 + 1, S.nl), o = S.loff[k]; fr[k] = lds_ld<bf8>(ub + o); }
    if constexpr (c0 < c1) P[c0] = pk2(D[c0 / 2][2 * (c0 & 1)], D[c0 / 2][2 * (c0 & 1) + 1]);
    if constexpr (c0 + 1 < c1) P[c0 + 1] = pk2(D[(c0 + 1) / 2][2 * ((c0 + 1) & 1)], D[(c0 + 1) / 2][2 * ((c0 + 1) & 1) + 1]);
    constexpr int yi = S.yat[0] == I ? 0 : S.yat[1] == I ? 1 : S.yat[2] == I ? 2 : I == S.n - 1 ? 3 : -1;
    if constexpr (yi >= 0) {
      const f4 y = acc[yi] + 0.0f;
      asm volatile("ds_write_b128 %0, %1 offset:%2\n\tds_write_b32 %3, %4 offset:%5" :: "v"(yaddr), "v"(y), "i"(yi * 1024), "v"(faddr), "v"(yaddr), "i"(yi * 16));
    }
    __builtin_amdgcn_sched_barrier(0);
    conv_step<HF, I + 1>(ub, yaddr, faddr, R, fr, D, P, acc);
  }
}
template <int HF, int L>
DEVI void conv_pre(u32 ub, bf8 (&fr)[96]) {
  if constexpr (L < PFD) { constexpr int k = lswap(L, 99), o = SchedOf<HF>::v.s.loff[k]; fr[k] = lds_ld<bf8>(ub + o); conv_pre<HF, L + 1>(ub, fr); }
}
template <int HF>
DEVI void conv(u32 ub, u32 yaddr, u32 faddr, const ConvRegs& R) {
  bf8 fr[96]; f4 D[40]; u32 P[80]; f4 acc[4];
  conv_pre<HF, 0>(ub, fr);
  __builtin_amdgcn_sched_barrier(0);
  conv_step<HF, 0>(ub, yaddr, faddr, R, fr, D, P, acc);
}

__global__ void __launch_bounds__(512, 2)
s4_moe_kernel(const u16* __restrict__ x, const u16* __restrict__ Are, const u16* __restrict__ Aim,
              const u16* __restrict__ Bre, const u16* __restrict__ Bim, const u16* __restrict__ Cre,
              const u16* __restrict__ Cim, const u16* __restrict__ ldt, const u16* __restrict__ dsk,
              const u16* __restrict__ wr, const u16* __restrict__ wg, const u16* __restrict__ wu,
              const u16* __restrict__ wd, u16* __restrict__ out, char* __restrict__ ws) {
  extern __shared__ __attribute__((aligned(16))) char smem[];
  const int lane = threadIdx.x & 63;
  const int wid = __builtin_amdgcn_readfirstlane(threadIdx.x >> 6);
  const int w = blockIdx.x, xc = w & 7, mm = w >> 3;
  const int cg = xc * 16 + (mm >> 1), hf = mm & 1;
  const bool prod = mm < 14;
  STAMP_DECL;
  STAMP(0);

  if (wid < 4) {
    __builtin_amdgcn_s_setprio(2);
    const int d = 4 * cg + wid;
    u2v xt0, xt1, xt2, xt3, xt4, xt5, xt6, xt7;
    const int R = xc * 4 + (mm >> 3), S = mm & 7;
    if (wid < 2) {
      const u32 xp = (64 * R + 8 * (lane & 7)) * 1024 + 128 * S + 64 * wid + 8 * (lane >> 3), xq = xp + 4096;
      GLD2_NT(xt0, xp, x, 0); GLD2_NT(xt1, xp, x, 1024); GLD2_NT(xt2, xp, x, 2048); GLD2_NT(xt3, xp, x, 3072);
      GLD2_NT(xt4, xq, x, 0); GLD2_NT(xt5, xq, x, 1024); GLD2_NT(xt6, xq, x, 2048); GLD2_NT(xt7, xq, x, 3072);
    }
    const u32 po = d * 256 + lane * 4;
    u32 p_ar, p_ai, p_br, p_bi, p_cr, p_ci, p_dt;
    GLDH(p_dt, 2 * d, ldt);
    GLD1_NT(p_ar, po, Are); GLD1_NT(p_ai, po, Aim); GLD1_NT(p_br, po, Bre); GLD1_NT(p_bi, po, Bim); GLD1_NT(p_cr, po, Cre); GLD1_NT(p_ci, po, Cim);
    if (wid >= 2) {
      u64 pc; asm volatile("s_getpc_b64 %0" : "=s"(pc));
      const u32 ln = (wid - 2) * 64 + lane;
      dma4((ln < CODE_LINES ? ln : 0) * 128, (const void*)pc, M_DUM);
      dma4(0u, out + 4 * cg, M_DUM);
    }
    if (lane < 32) lds_st<u4v>(UB + wid * UCH + lane * 16, (u4v){0, 0, 0, 0});
    asm volatile("s_waitcnt lgkmcnt(0)\n\ts_barrier" ::: "memory");
    STAMP(6);
    ConvRegs CR;
    CR.bands[0] = band(lane, 8, 2); CR.bands[1] = band(lane, 12, 1); CR.bands[2] = band(lane, 4, 1); CR.bands[3] = band(lane, 8, 4); CR.bands[4] = band(lane, 8, 3);
    const u32 kb = wid * KREG;
    const int hh = lane >> 5, j = lane & 31, od = lane & 1, wh = lane >> 2;
    u32 a0 = kb + 32 + 16 * j + 8 * hh;
    u32 a1 = kb + (lane < 36 ? 544 + 16 * lane : lane < 38 ? 16 * (lane - 36) : O_Y);
    u32 tA = kb + (hh ? (od ? O_TB + 32 : O_TA + 30) : (od ? O_TF + 32 : O_TE + 30)) + (j >> 1) * 2;
    u32 tB = hh ? kb + (od ? O_TD : O_TC) + 32 + (j >> 1) * 2 : tA + 64;
    constexpr u64 SAT = (u64)(O_TA + 30) | ((u64)(O_TE + 30) << 12) | ((u64)(O_TA + 94) << 24) | ((u64)(O_TC + 96) << 36) | ((u64)(O_TE + 158) << 48);
    u32 sa = kb + (lane < 20 ? (lane & 3) * (CS + 2) + (u32)((SAT >> (12 * (wh < 5 ? wh : 0))) & 0xfff) : O_Y);
    u32 zA = kb + (lane < 52 ? O_TA + lane * 16 : 1952);
    u32 cA = kb + 16 * lane, pA = lane ? cA - 8 : cA, cA2 = kb + (lane < 6 ? 1024 + 16 * lane : 1952);
    u32 m1 = lane == 32 ? 0xffffffffu : 0u, m0 = hh ? 0u : 0xffffffffu;
    u32 bn = kb + CS * (wh & 3) + 56 - 16 * (lane >> 4) + 8 * (lane & 3), bt = kb + CS * (wh & 3) - 8 * (lane >> 4) + 8 * (lane & 3);
    u32 ta = kb + lane * 4, fa = kb + j * TROW + hh * 16;
    asm volatile("" : "+v"(CR.bands[0]), "+v"(CR.bands[1]), "+v"(CR.bands[2]), "+v"(CR.bands[3]), "+v"(CR.bands[4]), "+v"(a0), "+v"(a1), "+v"(tA), "+v"(tB),
                 "+v"(sa), "+v"(zA), "+v"(cA), "+v"(pA), "+v"(cA2), "+v"(m1), "+v"(m0), "+v"(bn), "+v"(bt), "+v"(ta), "+v"(fa));
    STAMP(7);
    if (wid >= 2) {
      __builtin_amdgcn_s_sleep(SLP_W);
      const u32 row = mm * 16 + (lane >> 2);
      const u32 vo = row * 3584 + ((xc * 448) & ~127) + (lane & 3) * 128;
      dma4nt(vo, wid == 2 ? wg : wu, M_DUM);
    } else {
      u2v tg; u32 spins = 0;
      do { tg = lds_ldv<u2v>(M_TAG); } while (tg.y == 0 && ++spins < SPIN_CAP);
      STAMP(8);
      asm volatile("s_waitcnt vmcnt(7)" : "+v"(xt0), "+v"(xt1), "+v"(xt2), "+v"(xt3), "+v"(xt4), "+v"(xt5), "+v"(xt6), "+v"(xt7) :: "memory");
      STAMP(9);
      const u2v xt[8] = {xt0, xt1, xt2, xt3, xt4, xt5, xt6, xt7};
      u32 c = tg.x;
#pragma unroll
      for (int i = 0; i < 8; i++) c += xt[i].x + xt[i].y;
      const __amdgpu_buffer_rsrc_t rs = __builtin_amdgcn_make_buffer_rsrc(ws + W_XB + (R * 8 + S) * XBS, 0, XBS, 0x00020000);
      const u32 o = (8 * wid + (lane >> 3)) * 512 + (lane & 7) * 16;
#pragma unroll
      for (int i = 0; i < 4; i++)
        __builtin_amdgcn_raw_buffer_store_b128((u4v){xt[2 * i].x, xt[2 * i].y, xt[2 * i + 1].x, xt[2 * i + 1].y}, rs, o + i * 128, 0, 16);
      __builtin_amdgcn_raw_buffer_store_b32(c, rs, 8192 + wid * 256 + lane * 4, 0, 16);
    }
    asm volatile("s_cmp_lt_u32 %7, 2\n\ts_cbranch_scc1 1f\n\ts_waitcnt vmcnt(3)\n1:\n\ts_waitcnt vmcnt(5)"
                 : "+v"(p_ar), "+v"(p_ai), "+v"(p_br), "+v"(p_bi), "+v"(p_cr), "+v"(p_ci), "+v"(p_dt) : "s"(wid) : "memory", "scc");
    STAMP(1);

    const float dt = rbf(__builtin_amdgcn_exp2f(bl(p_dt) * 1.44269504f));
    f16v kd = {};
#pragma unroll 1
    for (int h = 0; h < 2; h++) {
      const u32 sh = 16 - 16 * h;
      const float a2 = dt * bh(p_ar << sh) * 1.44269504f, rv = dt * bh(p_ai << sh) * 0.159154943f;
      const f2 Bc = {dt * bh(p_br << sh), dt * bh(p_bi << sh)}, Cc = {bh(p_cr << sh), bh(p_ci << sh)};
      const f2 G = cmul(Cc, Bc, rot(Bc));
      const f2 w1 = cexp(a2, rv, 1.f), w256 = cexp(a2, rv, 256.f);
      f2 v8 = cexp(a2, -rv, 8.f);
      const f2 w1r = rot(w1), w256r = rot(w256);
      const f2 w2 = cmul(w1, w1, w1r), w2r = rot(w2), w4 = cmul(w2, w2, w2r), w4r = rot(w4);
      const f2 v16 = cmul(v8, v8, rot(v8)), v16r = rot(v16);
#define TW(r, v) lds_st<u32>(ta + (r) * TROW, pk2((v).x, (v).y))
      f2 fz = G, ge = v16, go = v8, fh = cmul(G, w256, w256r);
      TW(0, fz); TW(21, ((f2){1.f, 0.f})); TW(22, go); TW(23, ge); TW(8, fh);
      f2 t2 = cmul(fh, w2, w2r); TW(9, t2); t2 = cmul(t2, w2, w2r); TW(10, t2); t2 = cmul(t2, w2, w2r); TW(11, t2);
#pragma unroll
      for (int i = 1; i < 16; i++) {
        go = cmul(go, v16, v16r); TW(22 + 2 * i, go);
        if (i < 15) { ge = cmul(ge, v16, v16r); TW(23 + 2 * i, ge); }
        if (i < 8) { fz = cmul(fz, w1, w1r); TW(i, fz); }
        else {
          fh = cmul(fh, w256, w256r);
          if (i < 10) { TW(12 + (i - 8), fh); f2 t4 = cmul(fh, w4, w4r); TW(14 + (i - 8), t4); }
          else if (i < 15) TW(16 + (i - 10), fh);
        }
      }
      bf8 tf[16];
#pragma unroll
      for (int s = 0; s < 8; s++) { tf[2 * s] = lds_ld<bf8>(fa + 32 * s); tf[2 * s + 1] = lds_ld<bf8>(fa + 21 * TROW + 32 * s); }
      __builtin_amdgcn_sched_barrier(0);
#pragma unroll
      for (int s = 0; s < 8; s++) kd = __builtin_amdgcn_mfma_f32_32x32x16_bf16(tf[2 * s], tf[2 * s + 1], kd, 0, 0, 0);
    }
#pragma unroll
    for (int q = 0; q < 4; q++) lds_st<u4v>(zA + q * CS, (u4v){0, 0, 0, 0});
    STAMP(2);
    {
      lds_st<u2v>(a0, (u2v){pk2(kd[0], kd[1]), pk2(kd[2], kd[3])});
      const float en = DPPF(kd[4], 0x130, 0xf);
      const u32 e00 = pk2(kd[4], kd[4]);
      lds_st<u4v>(a1, (u4v){e00 & (m0 | m1), (pk2(kd[5], kd[6]) & m0) | (e00 & m1 & 0xffffu), pk2(kd[6], kd[6]) & m0, pk2(kd[7], en) & m0});
      const u4v c = lds_ld<u4v>(cA), c2 = lds_ld<u4v>(cA2);
      const u2v p = lds_ld<u2v>(pA), p2 = lds_ld<u2v>(cA2 - 8);
      const u32 v01 = hh ? pk2(kd[4], kd[5]) : pk2(kd[8], kd[9]), v23 = hh ? pk2(kd[6], kd[7]) : pk2(kd[10], kd[11]);
#pragma unroll
      for (int q = 0; q < 4; q++) {
        lds_st<u16>(tA + q * (CS + 2), (u16)v01); lds_st<u16>(tA + q * (CS + 2) + 32, (u16)(v01 >> 16));
        lds_st<u16>(tB + q * (CS + 2), (u16)v23); lds_st<u16>(tB + q * (CS + 2) + 32, (u16)(v23 >> 16));
      }
      const u32 k8 = pk2(kd[8], kd[8]);
      const u32 al = rl(k8, 0), k2048 = rl(k8, 32);
      lds_st<u16>(sa, (u16)(lane < 8 ? 0u : lane < 16 ? al : k2048));
      lds_st<u32>(M_AL + wid * 4, al << 16);
#define SHIFTS(c, p, A) do { \
      const u32 s0 = __builtin_amdgcn_alignbit(p.y, p.x, 16), s1 = __builtin_amdgcn_alignbit(c.x, p.y, 16), s2 = __builtin_amdgcn_alignbit(c.y, c.x, 16), \
                s3 = __builtin_amdgcn_alignbit(c.z, c.y, 16), s4 = __builtin_amdgcn_alignbit(c.w, c.z, 16); \
      lds_st<u4v>(A + CS, (u4v){s1, s2, s3, s4}); lds_st<u4v>(A + 2 * CS, (u4v){p.y, c.x, c.y, c.z}); lds_st<u4v>(A + 3 * CS, (u4v){s0, s1, s2, s3}); } while (0)
      SHIFTS(c, p, cA);
      SHIFTS(c2, p2, cA2);
    }
    MEMF();
#pragma unroll
    for (int a = 2; a <= 4; a++) {
      CR.T[2 * (a - 2)] = cat(trd(bt + O_TA + 32 * a - 32), trd(bt + O_TB + 32 * a - 32));
      CR.T[2 * (a - 2) + 1] = cat(trd(bt + O_TC + 32 * a - 32), trd(bt + O_TD + 32 * a - 32));
    }
#pragma unroll
    for (int a = 4; a <= 7; a++) CR.T[6 + a - 4] = cat(trd(bt + O_TE + 32 * a - 96), trd(bt + O_TF + 32 * a - 96));
#pragma unroll
    for (int m = 0; m < 17; m++) CR.An[m] = cat(trd(bn + 64 * m + 8), trd(bn + 64 * m));
    u32 ub = UB + wid * UCH + 32 + 32 * (lane & 15) + 16 * (lane >> 4);
    u32 ya = kb + O_Y + 64 * (lane & 15) + 16 * (lane >> 4), fl = M_YF + wid * 4;
    asm volatile("" : "+v"(ub), "+v"(ya), "+v"(fl));
    STAMP(3);
    asm volatile("s_waitcnt lgkmcnt(0)\n\ts_barrier" ::: "memory");
    STAMP(4);
    if (hf == 0) conv<0>(ub, ya, fl, CR); else conv<1>(ub, ya, fl, CR);
    STAMP(5);
    asm volatile("s_waitcnt vmcnt(0) lgkmcnt(0)" ::: "memory");
    STAMP_FLUSH();
    return;
  }

  const int cw = wid - 4;
  const int sp = xc * 32 + mm;
  u32 ep = 0;
  if (cw == 0) ep = __hip_atomic_load((GAS u32*)(ws + W_EP), __ATOMIC_RELAXED, __HIP_MEMORY_SCOPE_AGENT);
  const u4v xr0 = __builtin_nontemporal_load((const GAS u4v*)(x + (size_t)(8 * sp + 2 * cw) * 512) + lane);
  const u4v xr1 = __builtin_nontemporal_load((const GAS u4v*)(x + (size_t)(8 * sp + 2 * cw + 1) * 512) + lane);
  const u4v wv = ((const GAS u4v*)wr)[lane];
  const u2v dk = *(const GAS u2v*)(dsk + 4 * cg);
  if (cw == 3) lds_st<u32>(MB + lane * 4, 0u);
  asm volatile("s_waitcnt lgkmcnt(0)\n\ts_barrier" ::: "memory");
  u32 tag;
  if (cw == 0) {
    tag = __builtin_amdgcn_readfirstlane(ep) + 1u;
    if (lane == 0) lds_stv<u2v>(M_TAG, (u2v){tag, 1u});
  }
  {
    const bf2 ones = __builtin_bit_cast(bf2, 0x3F803F80u);
    float s0 = 0, q0 = 0, d0 = 0, s1 = 0, q1 = 0, d1 = 0, sw = 0;
#pragma unroll
    for (int i = 0; i < 4; i++) {
      const bf2 a = asbf2(xr0[i]), b = asbf2(xr1[i]), c = asbf2(wv[i]);
      s0 = __builtin_amdgcn_fdot2_f32_bf16(a, ones, s0, false); q0 = __builtin_amdgcn_fdot2_f32_bf16(a, a, q0, false); d0 = __builtin_amdgcn_fdot2_f32_bf16(a, c, d0, false);
      s1 = __builtin_amdgcn_fdot2_f32_bf16(b, ones, s1, false); q1 = __builtin_amdgcn_fdot2_f32_bf16(b, b, q1, false); d1 = __builtin_amdgcn_fdot2_f32_bf16(b, c, d1, false);
      sw = __builtin_amdgcn_fdot2_f32_bf16(c, ones, sw, false);
    }
    auto sw32 = [](float a, float b) { auto r = __builtin_amdgcn_permlane32_swap(__float_as_uint(a), __float_as_uint(b), false, false); return __uint_as_float(r[0]) + __uint_as_float(r[1]); };
    float s = sw32(s0, s1), q = sw32(q0, q1), dd = sw32(d0, d1); sw = sw32(sw, sw);
#define RED(v) v += DPPF(v, 0x128, 0xf);
    s += DPPF(s, 0x128, 0xf); q += DPPF(q, 0x128, 0xf); dd += DPPF(dd, 0x128, 0xf); sw += DPPF(sw, 0x128, 0xf);
    s += DPPF(s, 0x124, 0xf); q += DPPF(q, 0x124, 0xf); dd += DPPF(dd, 0x124, 0xf); sw += DPPF(sw, 0x124, 0xf);
    s += DPPF(s, 0x122, 0xf); q += DPPF(q, 0x122, 0xf); dd += DPPF(dd, 0x122, 0xf); sw += DPPF(sw, 0x122, 0xf);
    s += DPPF(s, 0x121, 0xf); q += DPPF(q, 0x121, 0xf); dd += DPPF(dd, 0x121, 0xf); sw += DPPF(sw, 0x121, 0xf);
    s += DPPF(s, 0x142, 0xa); q += DPPF(q, 0x142, 0xa); dd += DPPF(dd, 0x142, 0xa); sw += DPPF(sw, 0x142, 0xa);
    const float mean = s * (1.f / 512.f);
    const float var = fmaxf(q * (1.f / 512.f) - mean * mean, 0.f);
    const float mb = rbf(mean), sb = rbf(__builtin_amdgcn_sqrtf(rbf(rbf(var) + 1e-5f)));
    const float r = (dd - mb * sw) * __builtin_amdgcn_rcpf(sb);
    const u32 ms = pk2(mb, sb), rb = pk2(r, r) & 0xffffu;
    const u32 ms0 = rl(ms, 31), ms1 = rl(ms, 63), rr = rl(rb, 31) | (rl(rb, 63) << 16);
    if (cw != 0) {
      u2v tg; u32 spins = 0;
      do { tg = lds_ldv<u2v>(M_TAG); } while (tg.y == 0 && ++spins < SPIN_CAP);
      tag = __builtin_amdgcn_readfirstlane(tg.x);
    }
    const __amdgpu_buffer_rsrc_t rs = __builtin_amdgcn_make_buffer_rsrc(ws + W_ST, 0, 16384, 0x00020000);
    if (lane == 0) __builtin_amdgcn_raw_buffer_store_b128((u4v){ms0, ms1, rr, ms0 + ms1 + rr + tag + tag - 1u}, rs, cw * 4096 + sp * 16, 0, 16);
  }
  STAMP(1);
  const u32 chs = tag + tag - 1u;
  u4v su0, su1, su2, su3, xu0, xu1, xu2, xu3; u32 xk;
  const u32 so = W_ST + (64 * cw + lane) * 16;
  const u32 xb = W_XB + ((8 * cw + (lane >> 3)) * 8 + xc) * XBS;
  const u32 xo = xb + (mm >> 1) * 512 + (lane & 7) * 16, xko = xb + 8192 + (mm >> 1) * 32 + (lane & 7) * 4;
  const char* ws1 = ws + 4096; const char* ws2 = ws + 8192; const char* ws3 = ws + 12288;
#define LOAD_S() do { GLD4_SC1(su0, so, ws, 0); GLD4_SC1(su1, so, ws1, 0); GLD4_SC1(su2, so, ws2, 0); GLD4_SC1(su3, so, ws3, 0); } while (0)
#define LOAD_X() do { GLD4_SC1(xu0, xo, ws, 0); GLD4_SC1(xu1, xo, ws, 128); GLD4_SC1(xu2, xo, ws, 256); GLD4_SC1(xu3, xo, ws, 384); GLD1_SC1(xk, xko, ws, 0); } while (0)
#define OK_S() ((((su0.x + su0.y + su0.z + chs) ^ su0.w) | ((su1.x + su1.y + su1.z + chs) ^ su1.w) | ((su2.x + su2.y + su2.z + chs) ^ su2.w) | ((su3.x + su3.y + su3.z + chs) ^ su3.w)) == 0)
#define SUM4(v) (v.x + v.y + v.z + v.w)
#define OK_X() (SUM4(xu0) + SUM4(xu1) + SUM4(xu2) + SUM4(xu3) + tag == xk)
  __builtin_amdgcn_s_sleep(SLP_S);
  LOAD_S();
  __builtin_amdgcn_s_sleep(SLP_X);
  LOAD_X();
  const u32 fb = FB + cw * 12288;
  const int fp = xc * 14 + mm;
#define PULL() do { \
    const u32 go = (128 * cw + (lane >> 1)) * 3584 + fp * 32 + (lane & 1) * 16; \
    constexpr u32 GS = 32 * 3584 - 1024; \
    dma16x4(go, go + GS, go + 2 * GS, go + 3 * GS, wg, fb); \
    dma16x4(go, go + GS, go + 2 * GS, go + 3 * GS, wu, fb + 4096); \
    const u32 dof = (16 * fp + (lane >> 4)) * 1024 + 256 * cw + (lane & 15) * 16; \
    dma16x4(dof, dof + 3072, dof + 6144, dof + 9216, wd, fb + 8192); } while (0)
  asm volatile("s_waitcnt vmcnt(5)" : "+v"(su0), "+v"(su1), "+v"(su2), "+v"(su3) :: "memory");
  STAMP(2);
  for (u32 spins = 0; !__all(OK_S()) && spins < SPIN_CAP; spins++) {
    asm volatile("s_sleep 2\n\tglobal_load_dwordx4 %0, %4, %5 sc1\n\tglobal_load_dwordx4 %1, %4, %6 sc1\n\tglobal_load_dwordx4 %2, %4, %7 sc1\n\t"
                 "global_load_dwordx4 %3, %4, %8 sc1\n\ts_waitcnt vmcnt(0)"
                 : "=&v"(su0), "=&v"(su1), "=&v"(su2), "=&v"(su3) : "v"(so), "s"(ws), "s"(ws1), "s"(ws2), "s"(ws3) : "memory");
  }
  STAMP(3);
  u32 k1, k2;
  {
    const u32 ib = 0xffffu - (512 * cw + 8 * lane);
    auto key = [&](u32 b, int i) { u32 o = b ^ ((u32)((int)b >> 31) | 0x80000000u); return (o & 0xffff0000u) | (ib - i); };
    k1 = key(su0.z << 16, 0); k2 = 0;
    auto ins = [&](u32 k) { k2 = umax(k2, umin(k1, k)); k1 = umax(k1, k); };
    ins(key(su0.z & 0xffff0000u, 1)); ins(key(su1.z << 16, 2)); ins(key(su1.z & 0xffff0000u, 3)); ins(key(su2.z << 16, 4));
    ins(key(su2.z & 0xffff0000u, 5)); ins(key(su3.z << 16, 6)); ins(key(su3.z & 0xffff0000u, 7));
    const u32 K1 = wave_umax(k1), K2 = wave_umax(k1 == K1 ? k2 : k1);
    if (lane == 0) lds_stv<u2v>(M_TOP + cw * 8, (u2v){K1, K2});
    u4v ta, tb; u32 spins = 0;
    do { ta = lds_ldv<u4v>(M_TOP); tb = lds_ldv<u4v>(M_TOP + 16); } while ((ta.x == 0 || ta.z == 0 || tb.x == 0 || tb.z == 0) && ++spins < SPIN_CAP);
    const u32 a1 = umax(ta.x, ta.z), a2 = umax(umin(ta.x, ta.z), umax(ta.y, ta.w)), b1 = umax(tb.x, tb.z), b2 = umax(umin(tb.x, tb.z), umax(tb.y, tb.w));
    k1 = __builtin_amdgcn_readfirstlane(umax(a1, b1)); k2 = __builtin_amdgcn_readfirstlane(umax(umin(a1, b1), umax(a2, b2)));
  }
  const int t0 = (0xffffu - (k1 & 0xffffu)) & 2047, t1 = (0xffffu - (k2 & 0xffffu)) & 2047;
  u4v xs0, xs1, xs2, xs3;
  if (prod) {
    const u32 xso = ((lane & 1) ? t1 : t0) * 1024 + 256 * cw + 16 * (lane >> 4);
    GLD4(xs0, xso, x, 0); GLD4(xs1, xso, x, 64); GLD4(xs2, xso, x, 128); GLD4(xs3, xso, x, 192);
  }
  if (w == 0 && cw == 0 && lane == 0) __hip_atomic_store((GAS u32*)(ws + W_EP), tag, __ATOMIC_RELAXED, __HIP_MEMORY_SCOPE_AGENT);
  if ((t0 >> 3) == 64 * cw + lane) { lds_st<u4v>(M_SEL, (u4v){su0.x, su0.y, su1.x, su1.y}); lds_st<u4v>(M_SEL + 16, (u4v){su2.x, su2.y, su3.x, su3.y}); }
  if ((t1 >> 3) == 64 * cw + lane) { lds_st<u4v>(M_SEL + 32, (u4v){su0.x, su0.y, su1.x, su1.y}); lds_st<u4v>(M_SEL + 48, (u4v){su2.x, su2.y, su3.x, su3.y}); }
  float na[8], nb[8];
  {
    const u32 msv[8] = {su0.x, su0.y, su1.x, su1.y, su2.x, su2.y, su3.x, su3.y};
#pragma unroll
    for (int i = 0; i < 8; i++) { na[i] = __builtin_amdgcn_rcpf(bh(msv[i])); nb[i] = -bl(msv[i]) * na[i]; }
  }
  asm volatile("" : "+v"(na[0]), "+v"(na[1]), "+v"(na[2]), "+v"(na[3]), "+v"(na[4]), "+v"(na[5]), "+v"(na[6]), "+v"(na[7]),
               "+v"(nb[0]), "+v"(nb[1]), "+v"(nb[2]), "+v"(nb[3]), "+v"(nb[4]), "+v"(nb[5]), "+v"(nb[6]), "+v"(nb[7]));
  STAMP(4);
#define XW "4"
  asm volatile("s_cmp_lt_u32 %5, 111\n\ts_cbranch_scc1 1f\n\ts_waitcnt vmcnt(0)\n1:\n\ts_waitcnt vmcnt(" XW ")"
               : "+v"(xu0), "+v"(xu1), "+v"(xu2), "+v"(xu3), "+v"(xk) : "s"(w - 1) : "memory", "scc");
  for (u32 spins = 0; !__all(OK_X()) && spins < SPIN_CAP; spins++) {
    asm volatile("s_sleep 1\n\tglobal_load_dwordx4 %0, %5, %7 sc1\n\tglobal_load_dwordx4 %1, %5, %7 offset:128 sc1\n\tglobal_load_dwordx4 %2, %5, %7 offset:256 sc1\n\t"
                 "global_load_dwordx4 %3, %5, %7 offset:384 sc1\n\tglobal_load_dword %4, %6, %7 sc1\n\ts_waitcnt vmcnt(0)"
                 : "=&v"(xu0), "=&v"(xu1), "=&v"(xu2), "=&v"(xu3), "=&v"(xk) : "v"(xo), "v"(xko), "s"(ws) : "memory");
  }
  STAMP(5);
  u32 xa[8] = {xu0.x, xu0.z, xu1.x, xu1.z, xu2.x, xu2.z, xu3.x, xu3.z};
  u32 xcv[8] = {xu0.y, xu0.w, xu1.y, xu1.w, xu2.y, xu2.w, xu3.y, xu3.w};
  {
    u4v c0, c1, c2, c3;
#pragma unroll
    for (int i = 0; i < 4; i++) {
      const int r0 = 2 * i, r1 = 2 * i + 1;
      const f2 p0 = __builtin_elementwise_fma((f2){bl(xa[r0]), bh(xa[r0])}, (f2){na[r0], na[r0]}, (f2){nb[r0], nb[r0]});
      const f2 p1 = __builtin_elementwise_fma((f2){bl(xa[r1]), bh(xa[r1])}, (f2){na[r1], na[r1]}, (f2){nb[r1], nb[r1]});
      const f2 p2 = __builtin_elementwise_fma((f2){bl(xcv[r0]), bh(xcv[r0])}, (f2){na[r0], na[r0]}, (f2){nb[r0], nb[r0]});
      const f2 p3 = __builtin_elementwise_fma((f2){bl(xcv[r1]), bh(xcv[r1])}, (f2){na[r1], na[r1]}, (f2){nb[r1], nb[r1]});
      c0[i] = pk2(p0.x, p1.x); c1[i] = pk2(p0.y, p1.y); c2[i] = pk2(p2.x, p3.x); c3[i] = pk2(p2.y, p3.y);
    }
    const u32 ua = UB + 512 + (64 * cw + lane) * 16;
    lds_st<u4v>(ua, c0); lds_st<u4v>(ua + UCH, c1); lds_st<u4v>(ua + 2 * UCH, c2); lds_st<u4v>(ua + 3 * UCH, c3);
  }
  asm volatile("s_waitcnt lgkmcnt(0)\n\ts_barrier" ::: "memory");
  STAMP(6);

  if (prod) {
    PULL();
    const u32 msel = lds_ld<u32>(M_SEL + (lane & 1) * 32 + (((lane & 1) ? t1 : t0) & 7) * 4);
    const float a = __builtin_amdgcn_rcpf(bh(msel)), b = -bl(msel) * a;
    asm volatile("s_waitcnt vmcnt(0)" : "+v"(xs0), "+v"(xs1), "+v"(xs2), "+v"(xs3) :: "memory");
    STAMP(7);
    auto nrm = [&](u4v v) { u4v r;
#pragma unroll
      for (int i = 0; i < 4; i++) r[i] = pk2(fmaf(bl(v[i]), a, b), fmaf(bh(v[i]), a, b));
      return __builtin_bit_cast(bf8, r); };
    const bf8 xf[4] = {nrm(xs0), nrm(xs1), nrm(xs2), nrm(xs3)};
    const int g = lane >> 4, q = (lane >> 2) & 3, p = lane & 3;
    const u32 ga = fb + (8 * g + q) * 32 + 8 * p;
    f4 ag = {0, 0, 0, 0}, au = {0, 0, 0, 0};
#pragma unroll
    for (int s = 0; s < 4; s++) {
      ag = mma(cat(trd(ga + s * 1024), trd(ga + s * 1024 + 128)), xf[s], ag);
      au = mma(cat(trd(ga + 4096 + s * 1024), trd(ga + 4096 + s * 1024 + 128)), xf[s], au);
    }
    if ((lane & 15) < 2) {
      lds_st<f4>(M_PART + cw * 256 + (lane & 1) * 64 + g * 16, ag);
      lds_st<f4>(M_PART + cw * 256 + 128 + (lane & 1) * 64 + g * 16, au);
    }
    MEMF();
    if (lane == 0) lds_stv<u32>(M_FF + cw * 4, 1u);
    u4v ff; u32 spins = 0;
    do { ff = lds_ldv<u4v>(M_FF); } while ((ff.x & ff.y & ff.z & ff.w) == 0 && ++spins < SPIN_CAP);
    MEMF();
    f4 sg = {0, 0, 0, 0}, su = {0, 0, 0, 0};
#pragma unroll
    for (int v = 0; v < 4; v++) {
      sg += lds_ld<f4>(M_PART + v * 256 + (lane & 1) * 64 + g * 16);
      su += lds_ld<f4>(M_PART + v * 256 + 128 + (lane & 1) * 64 + g * 16);
    }
    float hv[4];
#pragma unroll
    for (int i = 0; i < 4; i++) { const float gg = rbf(sg[i]); hv[i] = rbf(gg * sigm(gg)) * rbf(su[i]); }
    const s4v hb = __builtin_bit_cast(s4v, ((u2v){pk2(hv[0], hv[1]), pk2(hv[2], hv[3])}));
    const u32 da = fb + 8192 + g * 1024 + q * 256 + 8 * p;
    const __amdgpu_buffer_rsrc_t rs = __builtin_amdgcn_make_buffer_rsrc(ws + W_FB + fp * 8192, 0, 8192, 0x00020000);
    const u32 fo = (32 * cw + g) * 64 + (lane & 1) * 32 + (lane & 2) * 8;
#pragma unroll
    for (int t = 0; t < 8; t++) {
      const f4 y = __builtin_amdgcn_mfma_f32_16x16x16bf16_1k(__builtin_bit_cast(s4v, trd(da + 32 * t)), hb, (f4){0, 0, 0, 0}, 0, 0, 0);
      const u4v o = {__float_as_uint((lane & 2) ? y[2] : y[0]), tag, __float_as_uint((lane & 2) ? y[3] : y[1]), tag};
      if ((lane & 12) == 0) __builtin_amdgcn_raw_buffer_store_b128(o, rs, fo + t * 256, 0, 16);
    }
    STAMP(8);
  }

  const int up = (cw & 1) ^ hf;
  const int tile = 2 * cw + up;
  const int tl = 8 * (lane & 31) + 4 * (lane >> 5), tr0 = 256 * tile + tl;
  float xv[4][4], uv[4][4], pre[4][4], gt[4][4];
#pragma unroll
  for (int r = 0; r < 4; r++) {
    auto s0 = __builtin_amdgcn_permlane32_swap(xa[r], xa[r + 4], false, false);
    auto s1 = __builtin_amdgcn_permlane32_swap(xcv[r], xcv[r + 4], false, false);
    const u32 e0 = up ? s0[1] : s0[0], e1 = up ? s1[1] : s1[0];
    xv[0][r] = bl(e0); xv[1][r] = bh(e0); xv[2][r] = bl(e1); xv[3][r] = bh(e1);
  }
  {
    const f4 alv = lds_ld<f4>(M_AL);
    const float dv[4] = {bl(dk.x), bh(dk.x), bl(dk.y), bh(dk.y)};
    const u32 ua = UB + 512 + tr0 * 2;
    const u32 ca = tile >= 4 ? ua - 2064 : UB;
#pragma unroll
    for (int c = 0; c < 4; c++) {
      const u2v a = lds_ld<u2v>(ua + c * UCH), p = lds_ld<u2v>(ca + c * UCH), n = lds_ld<u2v>(ca + c * UCH + 8);
      uv[c][0] = bl(a.x); uv[c][1] = bh(a.x); uv[c][2] = bl(a.y); uv[c][3] = bh(a.y);
      pre[c][0] = uv[c][0] * dv[c] - alv[c] * bh(p.y); pre[c][1] = uv[c][1] * dv[c] - alv[c] * bl(n.x);
      pre[c][2] = uv[c][2] * dv[c] - alv[c] * bh(n.x); pre[c][3] = uv[c][3] * dv[c] - alv[c] * bl(n.y);
#pragma unroll
      for (int r = 0; r < 4; r++) gt[c][r] = uv[c][r] * sigm(uv[c][r]);
    }
  }
  STAMP(9);
  {
    u4v yf; u32 spins = 0;
    do { __builtin_amdgcn_s_sleep(1); yf = lds_ldv<u4v>(M_YF + cw * 16); } while (umin(umin(yf.x, yf.y), umin(yf.z, yf.w)) == 0 && ++spins < SPIN_CAP);
  }
  MEMF();
  STAMP(10);
#pragma unroll
  for (int c = 0; c < 4; c++) {
    const f4 y = lds_ld<f4>(c * KREG + O_Y + cw * 1024 + tl * 4);
#pragma unroll
    for (int r = 0; r < 4; r++) pre[c][r] += y[r];
  }
  const bool late = (tr0 >> 2) == (t0 >> 2) || (tr0 >> 2) == (t1 >> 2);
  char* op = (char*)out + (size_t)tr0 * 1024 + cg * 8;
  if (!late) {
#pragma unroll
    for (int r = 0; r < 4; r++)
      *(GAS u2v*)(op + r * 1024) = (u2v){pk2(fmaf(pre[0][r], gt[0][r], xv[0][r]), fmaf(pre[1][r], gt[1][r], xv[1][r])),
                                         pk2(fmaf(pre[2][r], gt[2][r], xv[2][r]), fmaf(pre[3][r], gt[3][r], xv[3][r]))};
  }
  STAMP(11);
  if ((t0 >> 8) == tile || (t1 >> 8) == tile) {
#pragma unroll 1
    for (int k = 0; k < 2; k++) {
      const int tk = k ? t1 : t0; const u32 kk = k ? k2 : k1;
      if ((tk >> 8) != tile) continue;
      const u32 fo = W_FB + (lane >> 1) * 8192 + cg * 64 + k * 32 + (lane & 1) * 16;
      const u32 fo3 = lane < 32 ? fo + 96 * 8192 : fo;
      u4v g0, g1, g2, g3;
      for (u32 spins = 0; spins < SPIN_CAP; spins++) {
        asm volatile("global_load_dwordx4 %0, %4, %8 sc1\n\tglobal_load_dwordx4 %1, %5, %8 sc1\n\tglobal_load_dwordx4 %2, %6, %8 sc1\n\t"
                     "global_load_dwordx4 %3, %7, %8 sc1\n\ts_waitcnt vmcnt(0)"
                     : "=&v"(g0), "=&v"(g1), "=&v"(g2), "=&v"(g3) : "v"(fo), "v"(fo + 32 * 8192), "v"(fo + 64 * 8192), "v"(fo3), "s"(ws) : "memory");
        if (__all(((g0.y ^ tag) | (g0.w ^ tag) | (g1.y ^ tag) | (g1.w ^ tag) | (g2.y ^ tag) | (g2.w ^ tag) | (g3.y ^ tag) | (g3.w ^ tag)) == 0)) break;
        __builtin_amdgcn_s_sleep(1);
      }
      float v0 = __uint_as_float(g0.x) + __uint_as_float(g1.x) + __uint_as_float(g2.x) + (lane < 32 ? __uint_as_float(g3.x) : 0.f);
      float v1 = __uint_as_float(g0.z) + __uint_as_float(g1.z) + __uint_as_float(g2.z) + (lane < 32 ? __uint_as_float(g3.z) : 0.f);
      v0 += DPPF(v0, 0x128, 0xf); v1 += DPPF(v1, 0x128, 0xf); v0 += DPPF(v0, 0x124, 0xf); v1 += DPPF(v1, 0x124, 0xf);
      v0 += DPPF(v0, 0x122, 0xf); v1 += DPPF(v1, 0x122, 0xf);
      float yv[4];
      {
        const u32 a = __float_as_uint(v0), b = __float_as_uint(v1);
        yv[0] = (__uint_as_float(rl(a, 0)) + __uint_as_float(rl(a, 16))) + (__uint_as_float(rl(a, 32)) + __uint_as_float(rl(a, 48)));
        yv[1] = (__uint_as_float(rl(b, 0)) + __uint_as_float(rl(b, 16))) + (__uint_as_float(rl(b, 32)) + __uint_as_float(rl(b, 48)));
        yv[2] = (__uint_as_float(rl(a, 1)) + __uint_as_float(rl(a, 17))) + (__uint_as_float(rl(a, 33)) + __uint_as_float(rl(a, 49)));
        yv[3] = (__uint_as_float(rl(b, 1)) + __uint_as_float(rl(b, 17))) + (__uint_as_float(rl(b, 33)) + __uint_as_float(rl(b, 49)));
      }
      const u32 ob = kk & 0xffff0000u;
      const float rs = rbf(sigm(__uint_as_float((ob & 0x80000000u) ? (ob ^ 0x80000000u) : (~ob & 0xffff0000u))));
#pragma unroll
      for (int c = 0; c < 4; c++) {
        const float upd = rbf(rbf(yv[c]) * rs);
#pragma unroll
        for (int r = 0; r < 4; r++) {
          const float b = rbf(uv[c][r] + upd);
          gt[c][r] = (tr0 + r == tk) ? b * sigm(b) : gt[c][r];
        }
      }
    }
    if (late) {
#pragma unroll
      for (int r = 0; r < 4; r++)
        *(GAS u2v*)(op + r * 1024) = (u2v){pk2(fmaf(pre[0][r], gt[0][r], xv[0][r]), fmaf(pre[1][r], gt[1][r], xv[1][r])),
                                           pk2(fmaf(pre[2][r], gt[2][r], xv[2][r]), fmaf(pre[3][r], gt[3][r], xv[3][r]))};
    }
    STAMP(12);
  }
  asm volatile("s_waitcnt vmcnt(0)" ::: "memory");
  STAMP(13);
  STAMP_FLUSH();
}

extern "C" void kernel_launch(void* const* d_in, const int* in_sizes, int n_in, void* d_out, int out_size,
                              void* d_ws, size_t ws_size, hipStream_t stream) {
  (void)hipFuncSetAttribute((const void*)s4_moe_kernel, hipFuncAttributeMaxDynamicSharedMemorySize, LDS_BYTES);
  s4_moe_kernel<<<256, 512, LDS_BYTES, stream>>>(
      (const u16*)d_in[0], (const u16*)d_in[1], (const u16*)d_in[2], (const u16*)d_in[3], (const u16*)d_in[4],
      (const u16*)d_in[5], (const u16*)d_in[6], (const u16*)d_in[7], (const u16*)d_in[8], (const u16*)d_in[10],
      (const u16*)d_in[11], (const u16*)d_in[12], (const u16*)d_in[13], (u16*)d_out, (char*)d_ws);
}
```

```cpp
#define KMODE 0
#define PULL_LATE
#include <hip/hip_runtime.h>
#include <hip/hip_bf16.h>
#include <stdint.h>

typedef unsigned short u16;
typedef unsigned int u32;
typedef unsigned long long u64;
typedef float f2 __attribute__((ext_vector_type(2)));
typedef float f4 __attribute__((ext_vector_type(4)));
typedef float f16v __attribute__((ext_vector_type(16)));
typedef u32 u2v __attribute__((ext_vector_type(2)));
typedef u32 u4v __attribute__((ext_vector_type(4)));
typedef __bf16 bf2 __attribute__((ext_vector_type(2)));
typedef __bf16 bf4 __attribute__((ext_vector_type(4)));
typedef __bf16 bf8 __attribute__((ext_vector_type(8)));
typedef short s4v __attribute__((ext_vector_type(4)));
#define LAS __attribute__((address_space(3)))
#define GAS __attribute__((address_space(1)))
#define DEVI __device__ __forceinline__

#define SLP_S 10
#define SLP_X 4
#define SLP_W 20
#define SPIN_CAP (1u << 18)

constexpr int KREG = 14592;
constexpr int TROW = 272;
constexpr int CS = 2112;
constexpr int O_TA = 1120, O_TB = 1248, O_TC = 1376, O_TD = 1504, O_TE = 1632, O_TF = 1792;
constexpr int O_Y = 8448;
constexpr int UB = 4 * KREG;
constexpr int UCH = 4608;
constexpr int FB = UB + 4 * UCH;
constexpr int MB = FB + 49152;
constexpr int M_TAG = MB;
constexpr int M_TOP = MB + 16;
constexpr int M_YF = MB + 64;
constexpr int M_FF = MB + 128;
constexpr int M_AL = MB + 144;
constexpr int M_SEL = MB + 192;
constexpr int M_PART = MB + 256;
constexpr int M_DUM = MB + 1280;
constexpr int LDS_BYTES = MB + 1536;

constexpr u32 W_EP = 0;
constexpr u32 W_ST = 4096;
constexpr u32 W_XB = 65536;
constexpr u32 XBS = 8704;
constexpr u32 W_FB = 4u << 20;
constexpr u32 W_STAMP = 8u << 20;

#define STAMP(i) do {} while (0)
#define STAMP_DECL do {} while (0)
#define STAMP_FLUSH() do {} while (0)
#define CODE_LINES 64

DEVI float bl(u32 v) { return __uint_as_float(v << 16); }
DEVI float bh(u32 v) { return __uint_as_float(v & 0xffff0000u); }
DEVI u32 pk2(float a, float b) { f2 v = {a, b}; return __builtin_bit_cast(u32, __builtin_convertvector(v, bf2)); }
DEVI bf2 asbf2(u32 v) { return __builtin_bit_cast(bf2, v); }
DEVI float rbf(float a) { return bl(pk2(a, a)); }
DEVI f2 cmul(f2 x, f2 y, f2 yr) { return __builtin_elementwise_fma(x.yy, yr, x.xx * y); }
DEVI f2 rot(f2 y) { return (f2){-y.y, y.x}; }
DEVI f2 cexp(float a2, float rev, float l) {
  float e = __builtin_amdgcn_exp2f(a2 * l), p = __builtin_amdgcn_fractf(rev * l);
  return (f2){e * __builtin_amdgcn_cosf(p), e * __builtin_amdgcn_sinf(p)};
}
DEVI float sigm(float v) { return __builtin_amdgcn_rcpf(1.f + __builtin_amdgcn_exp2f(v * -1.44269504f)); }
template <class T> DEVI T lds_ld(u32 a) { return *(const LAS T*)(uintptr_t)a; }
template <class T> DEVI void lds_st(u32 a, T v) { *(LAS T*)(uintptr_t)a = v; }
template <class T> DEVI T lds_ldv(u32 a) { return *(const volatile LAS T*)(uintptr_t)a; }
template <class T> DEVI void lds_stv(u32 a, T v) { *(volatile LAS T*)(uintptr_t)a = v; }
DEVI bf4 trd(u32 a) { return __builtin_amdgcn_ds_read_tr16_b64_v4bf16((LAS bf4*)(uintptr_t)a); }
DEVI bf8 cat(bf4 a, bf4 b) { return __builtin_shufflevector(a, b, 0, 1, 2, 3, 4, 5, 6, 7); }
DEVI f4 mma(bf8 a, bf8 b, f4 c) { return __builtin_amdgcn_mfma_f32_16x16x32_bf16(a, b, c, 0, 0, 0); }
DEVI u32 rl(u32 v, int l) { return __builtin_amdgcn_readlane(v, l); }
#define DPPF(v, ctl, rm) __uint_as_float(__builtin_amdgcn_update_dpp(0u, __float_as_uint(v), ctl, rm, 0xf, true))
#define DPPU(v, ctl, rm) __builtin_amdgcn_update_dpp(0u, v, ctl, rm, 0xf, true)
DEVI u32 umax(u32 a, u32 b) { return a > b ? a : b; }
DEVI u32 umin(u32 a, u32 b) { return a < b ? a : b; }
DEVI u32 wave_umax(u32 v) {
  v = umax(v, DPPU(v, 0x128, 0xf)); v = umax(v, DPPU(v, 0x124, 0xf)); v = umax(v, DPPU(v, 0x122, 0xf)); v = umax(v, DPPU(v, 0x121, 0xf));
  v = umax(v, DPPU(v, 0x142, 0xa)); v = umax(v, DPPU(v, 0x143, 0xc));
  return rl(v, 63);
}
#define MEMF() asm volatile("" ::: "memory")

#define GLD4_SC1(d, vo, base, imm) asm volatile("global_load_dwordx4 %0, %1, %2 offset:%3 sc1" : "=v"(d) : "v"(vo), "s"(base), "i"(imm) : "memory")
#define GLD1_SC1(d, vo, base, imm) asm volatile("global_load_dword %0, %1, %2 offset:%3 sc1" : "=v"(d) : "v"(vo), "s"(base), "i"(imm) : "memory")
#define GLD2_NT(d, vo, base, imm) asm volatile("global_load_dwordx2 %0, %1, %2 offset:%3 nt" : "=v"(d) : "v"(vo), "s"(base), "i"(imm) : "memory")
#define GLD1_NT(d, vo, base) asm volatile("global_load_dword %0, %1, %2 nt" : "=v"(d) : "v"(vo), "s"(base) : "memory")
#define GLDH(d, vo, base) asm volatile("global_load_ushort %0, %1, %2" : "=v"(d) : "v"(vo), "s"(base) : "memory")
#define GLD4(d, vo, base, imm) asm volatile("global_load_dwordx4 %0, %1, %2 offset:%3" : "=v"(d) : "v"(vo), "s"(base), "i"(imm) : "memory")
DEVI void dma16(u32 vo, const void* base, u32 ldsdst) {
  u32 keep;
  asm volatile("s_mov_b32 %0, m0\n\ts_mov_b32 m0, %3\n\ts_nop 0\n\tglobal_load_lds_dwordx4 %1, %2\n\ts_mov_b32 m0, %0" : "=&s"(keep) : "v"(vo), "s"(base), "s"(ldsdst) : "memory");
}
DEVI void dma16x4(u32 v0, u32 v1, u32 v2, u32 v3, const void* base, u32 ldsdst) {
  u32 keep;
  asm volatile("s_mov_b32 %0, m0\n\ts_mov_b32 m0, %6\n\ts_nop 0\n\tglobal_load_lds_dwordx4 %1, %5\n\tglobal_load_lds_dwordx4 %2, %5 offset:1024\n\t"
               "global_load_lds_dwordx4 %3, %5 offset:2048\n\tglobal_load_lds_dwordx4 %4, %5 offset:3072\n\ts_mov_b32 m0, %0"
               : "=&s"(keep) : "v"(v0), "v"(v1), "v"(v2), "v"(v3), "s"(base), "s"(ldsdst) : "memory");
}
DEVI void dma4nt(u32 vo, const void* base, u32 ldsdst) {
  u32 keep;
  asm volatile("s_mov_b32 %0, m0\n\ts_mov_b32 m0, %3\n\ts_nop 0\n\tglobal_load_lds_dword %1, %2 nt\n\ts_mov_b32 m0, %0" : "=&s"(keep) : "v"(vo), "s"(base), "s"(ldsdst) : "memory");
}
DEVI void dma4(u32 vo, const void* base, u32 ldsdst) {
  u32 keep;
  asm volatile("s_mov_b32 %0, m0\n\ts_mov_b32 m0, %3\n\ts_nop 0\n\tglobal_load_lds_dword %1, %2\n\ts_mov_b32 m0, %0" : "=&s"(keep) : "v"(vo), "s"(base), "s"(ldsdst) : "memory");
}

DEVI bf8 band(int lane, int co, int hw) {
  int lo = (lane & 15) + co - hw - 8 * (lane >> 4);
  u32 m = (u32)(((u64)((1u << (2 * hw + 1)) - 1) << (lo + 24)) >> 24);
  u4v r;
#pragma unroll
  for (int i = 0; i < 4; i++) r[i] = ((m >> (2 * i)) & 1u) * 0x3F80u + ((m >> (2 * i + 1)) & 1u) * 0x3F800000u;
  return __builtin_bit_cast(bf8, r);
}

struct Sched {
  int n, nl, nd;
  int kind[128], p0[128], p1[128], ld[128], tile[128];
  int loff[96];
  int dsrc[40];
  int last[4];
  int lcum[129], ccum[129], yat[4];
};
constexpr int PFD = 8;
constexpr int lswap(int k, int n) { return (k ^ 1) < n ? (k ^ 1) : k; }
constexpr int tile_of(int hf, int i) { return hf == 0 ? (i == 0 ? 0 : i == 1 ? 3 : i == 2 ? 4 : 7) : (i == 0 ? 1 : i == 1 ? 2 : i == 2 ? 5 : 6); }
struct Sched2 { Sched s; int dmap[32]; };
constexpr Sched2 make_sched(int hf) {
  Sched2 r{}; Sched& s = r.s;
  for (int i = 0; i < 4; i++) {
    const int q = tile_of(hf, i), pq = i ? tile_of(hf, i - 1) : -1;
    for (int e = 0; e < 6; e++) {
      const bool n2 = (e <= q - 2) && (e >= q - 4) && !((e <= pq - 2) && (e >= pq - 4));
      const bool n3 = (e <= q - 4) && !(e <= pq - 4);
      if (!(n2 || n3)) continue;
      const int l24 = s.nl++, l16 = s.nl++;
      s.loff[l24] = 64 * (8 * e + 7) - 16; s.loff[l16] = 64 * (8 * e + 7);
      const int bsel[6] = {0, 0, 1, 2, 3, 4}, lsel[6] = {l24, l16, l16, l16, l24, l16};
      const int dm[6] = {4 * e, 4 * e + 1, 4 * e + 2, 4 * e + 3, 24 + 2 * e, 24 + 2 * e + 1};
      for (int k = 0; k < 6; k++) {
        if (k < 4 ? !n2 : !n3) continue;
        s.kind[s.n] = 1; s.p0[s.n] = bsel[k]; s.p1[s.n] = s.nd; s.ld[s.n] = lsel[k]; s.tile[s.n] = i;
        r.dmap[dm[k]] = s.nd; s.dsrc[s.nd++] = s.n; s.n++;
      }
    }
    for (int m = 0; m < 17; m++) {
      const int pp = 8 * q - m;
      if (pp < -7) continue;
      s.loff[s.nl] = 64 * (pp + 7);
      s.kind[s.n] = 0; s.p0[s.n] = m; s.ld[s.n] = s.nl++; s.tile[s.n] = i; s.n++;
    }
    for (int a = 2; a <= 4; a++)
      for (int h = 0; h < 2; h++)
        if (q - a >= 0) { s.kind[s.n] = 2; s.p0[s.n] = 2 * (q - a) + h; s.p1[s.n] = 2 * (a - 2) + h; s.ld[s.n] = -1; s.tile[s.n] = i; s.n++; }
    for (int a = 4; a <= 7; a++)
      if (q - a >= 0) { s.kind[s.n] = 2; s.p0[s.n] = 12 + (q - a); s.p1[s.n] = 6 + (a - 4); s.ld[s.n] = -1; s.tile[s.n] = i; s.n++; }
    s.last[i] = s.n - 1;
    s.yat[i] = s.n + 1;
  }
  int nl = PFD, nc = 0, mx = 0;
  s.lcum[0] = nl; s.ccum[0] = 0;
  for (int i = 0; i < s.n; i++) {
    if (s.ld[i] > mx) mx = s.ld[i];
    int slots = 2;
    if (nl < s.nl && nl <= (mx | 1) + PFD - 1) { nl++; slots--; }
    while (slots > 0 && nc < 2 * s.nd && s.dsrc[nc / 2] <= i - 2) { nc++; slots--; }
    if (slots > 0 && nl < s.nl && nl <= (mx | 1) + PFD - 1) { nl++; slots--; }
    s.lcum[i + 1] = nl; s.ccum[i + 1] = nc;
  }
  return r;
}
template <int HF> struct SchedOf { static constexpr Sched2 v = make_sched(HF); };

struct ConvRegs { bf8 An[17], T[10], bands[5]; };
template <int HF, int I>
DEVI void conv_step(u32 ub, u32 yaddr, u32 faddr, const ConvRegs& R, bf8 (&fr)[96], f4 (&D)[40], u32 (&P)[80], f4 (&acc)[4]) {
  constexpr const Sched2& S2 = SchedOf<HF>::v; constexpr const Sched& S = S2.s;
  if constexpr (I < S.n) {
    const f4 z = {0.f, 0.f, 0.f, 0.f};
    constexpr int t = S.tile[I], p0 = S.p0[I], p1 = S.p1[I], ld = S.ld[I];
    if constexpr (S.kind[I] == 0) acc[t] = mma(R.An[p0], fr[ld], p0 == 0 ? z : acc[t]);
    else if constexpr (S.kind[I] == 1) D[p1] = mma(fr[ld], R.bands[p0], z);
    else {
      constexpr int d0 = S2.dmap[2 * S.p0[I]], d1 = S2.dmap[2 * S.p0[I] + 1];
      static_assert(S.ccum[I] >= 2 * d0 + 2 && S.ccum[I] >= 2 * d1 + 2, "window not converted in time");
      acc[t] = mma(__builtin_bit_cast(bf8, ((u4v){P[2 * d0], P[2 * d0 + 1], P[2 * d1], P[2 * d1 + 1]})), R.T[p1], acc[t]);
    }
    __builtin_amdgcn_sched_barrier(0);
    constexpr int l0 = S.lcum[I], l1 = S.lcum[I + 1], c0 = S.ccum[I], c1 = S.ccum[I + 1];
    if constexpr (l0 < l1) { constexpr int k = lswap(l0, S.nl), o = S.loff[k]; fr[k] = lds_ld<bf8>(ub + o); }
    if constexpr (l0 + 1 < l1) { constexpr int k = lswap(l0 + 1, S.nl), o = S.loff[k]; fr[k] = lds_ld<bf8>(ub + o); }
    if constexpr (c0 < c1) P[c0] = pk2(D[c0 / 2][2 * (c0 & 1)], D[c0 / 2][2 * (c0 & 1) + 1]);
    if constexpr (c0 + 1 < c1) P[c0 + 1] = pk2(D[(c0 + 1) / 2][2 * ((c0 + 1) & 1)], D[(c0 + 1) / 2][2 * ((c0 + 1) & 1) + 1]);
    constexpr int yi = S.yat[0] == I ? 0 : S.yat[1] == I ? 1 : S.yat[2] == I ? 2 : I == S.n - 1 ? 3 : -1;
    if constexpr (yi >= 0) {
      const f4 y = acc[yi] + 0.0f;
      asm volatile("ds_write_b128 %0, %1 offset:%2\n\tds_write_b32 %3, %4 offset:%5" :: "v"(yaddr), "v"(y), "i"(yi * 1024), "v"(faddr), "v"(yaddr), "i"(yi * 16));
    }
    __builtin_amdgcn_sched_barrier(0);
    conv_step<HF, I + 1>(ub, yaddr, faddr, R, fr, D, P, acc);
  }
}
template <int HF, int L>
DEVI void conv_pre(u32 ub, bf8 (&fr)[96]) {
  if constexpr (L < PFD) { constexpr int k = lswap(L, 99), o = SchedOf<HF>::v.s.loff[k]; fr[k] = lds_ld<bf8>(ub + o); conv_pre<HF, L + 1>(ub, fr); }
}
template <int HF>
DEVI void conv(u32 ub, u32 yaddr, u32 faddr, const ConvRegs& R) {
  bf8 fr[96]; f4 D[40]; u32 P[80]; f4 acc[4];
  conv_pre<HF, 0>(ub, fr);
  __builtin_amdgcn_sched_barrier(0);
  conv_step<HF, 0>(ub, yaddr, faddr, R, fr, D, P, acc);
}

__global__ void __launch_bounds__(512, 2)
s4_moe_kernel(const u16* x_, const u16* Are_, const u16* Aim_, const u16* Bre_, const u16* Bim_, const u16* Cre_,
              const u16* Cim_, const u16* ldt_, const u16* dsk_, const u16* wr_, const u16* wg_, const u16* wu_,
              const u16* wd_, u16* out_, char* ws_, u64 pad_) {
  extern __shared__ __attribute__((aligned(16))) char smem[];
  const int lane = threadIdx.x & 63;
  const int wid = __builtin_amdgcn_readfirstlane(threadIdx.x >> 6);
  const int w = blockIdx.x, xc = w & 7, mm = w >> 3;
  const int cg = xc * 16 + (mm >> 1), hf = mm & 1;
  const bool prod = mm < 14;
  STAMP_DECL;
  STAMP(0);
  typedef u32 u16v __attribute__((ext_vector_type(16)));
  u16v ka, kb_;
  asm volatile("s_load_dwordx16 %0, %2, 0x0\n\ts_load_dwordx16 %1, %2, 0x40\n\ts_waitcnt lgkmcnt(0)" : "=&s"(ka), "=&s"(kb_) : "s"(__builtin_amdgcn_kernarg_segment_ptr()) : "memory");
  STAMP(14);
#define KP(T, v, i) ((T)(((u64)v[2 * (i) + 1] << 32) | v[2 * (i)]))
  const u16* x = KP(const u16*, ka, 0); const u16* Are = KP(const u16*, ka, 1); const u16* Aim = KP(const u16*, ka, 2); const u16* Bre = KP(const u16*, ka, 3);
  const u16* Bim = KP(const u16*, ka, 4); const u16* Cre = KP(const u16*, ka, 5); const u16* Cim = KP(const u16*, ka, 6); const u16* ldt = KP(const u16*, ka, 7);
  const u16* dsk = KP(const u16*, kb_, 0); const u16* wr = KP(const u16*, kb_, 1); const u16* wg = KP(const u16*, kb_, 2); const u16* wu = KP(const u16*, kb_, 3);
  const u16* wd = KP(const u16*, kb_, 4); u16* out = KP(u16*, kb_, 5); char* ws = KP(char*, kb_, 6);

  if (wid < 4) {
    __builtin_amdgcn_s_setprio(2);
    const int d = 4 * cg + wid;
    u2v xt0, xt1, xt2, xt3, xt4, xt5, xt6, xt7;
    const int R = xc * 4 + (mm >> 3), S = mm & 7;
    if (wid < 2) {
      const u32 xp = (64 * R + 8 * (lane & 7)) * 1024 + 128 * S + 64 * wid + 8 * (lane >> 3), xq = xp + 4096;
      GLD2_NT(xt0, xp, x, 0); GLD2_NT(xt1, xp, x, 1024); GLD2_NT(xt2, xp, x, 2048); GLD2_NT(xt3, xp, x, 3072);
      GLD2_NT(xt4, xq, x, 0); GLD2_NT(xt5, xq, x, 1024); GLD2_NT(xt6, xq, x, 2048); GLD2_NT(xt7, xq, x, 3072);
    }
    const u32 po = d * 256 + lane * 4;
    u32 p_ar, p_ai, p_br, p_bi, p_cr, p_ci, p_dt;
    GLDH(p_dt, 2 * d, ldt);
    GLD1_NT(p_ar, po, Are); GLD1_NT(p_ai, po, Aim); GLD1_NT(p_br, po, Bre); GLD1_NT(p_bi, po, Bim); GLD1_NT(p_cr, po, Cre); GLD1_NT(p_ci, po, Cim);
    if (wid >= 2) {
      u64 pc; asm volatile("s_getpc_b64 %0" : "=s"(pc));
      const u32 ln = (wid - 2) * 64 + lane;
      dma4((ln < CODE_LINES ? ln : 0) * 128, (const void*)pc, M_DUM);
      dma4(0u, out + 4 * cg, M_DUM);
    }
    if (lane < 32) lds_st<u4v>(UB + wid * UCH + lane * 16, (u4v){0, 0, 0, 0});
    asm volatile("s_waitcnt lgkmcnt(0)\n\ts_barrier" ::: "memory");
    STAMP(6);
    ConvRegs CR;
    CR.bands[0] = band(lane, 8, 2); CR.bands[1] = band(lane, 12, 1); CR.bands[2] = band(lane, 4, 1); CR.bands[3] = band(lane, 8, 4); CR.bands[4] = band(lane, 8, 3);
    const u32 kb = wid * KREG;
    const int hh = lane >> 5, j = lane & 31, od = lane & 1, wh = lane >> 2;
    u32 a0 = kb + 32 + 16 * j + 8 * hh;
    u32 a1 = kb + (lane < 36 ? 544 + 16 * lane : lane < 38 ? 16 * (lane - 36) : O_Y);
    u32 tA = kb + (hh ? (od ? O_TB + 32 : O_TA + 30) : (od ? O_TF + 32 : O_TE + 30)) + (j >> 1) * 2;
    u32 tB = hh ? kb + (od ? O_TD : O_TC) + 32 + (j >> 1) * 2 : tA + 64;
    constexpr u64 SAT = (u64)(O_TA + 30) | ((u64)(O_TE + 30) << 12) | ((u64)(O_TA + 94) << 24) | ((u64)(O_TC + 96) << 36) | ((u64)(O_TE + 158) << 48);
    u32 sa = kb + (lane < 20 ? (lane & 3) * (CS + 2) + (u32)((SAT >> (12 * (wh < 5 ? wh : 0))) & 0xfff) : O_Y);
    u32 zA = kb + (lane < 52 ? O_TA + lane * 16 : 1952);
    u32 cA = kb + 16 * lane, pA = lane ? cA - 8 : cA, cA2 = kb + (lane < 6 ? 1024 + 16 * lane : 1952);
    u32 m1 = lane == 32 ? 0xffffffffu : 0u, m0 = hh ? 0u : 0xffffffffu;
    u32 bn = kb + CS * (wh & 3) + 56 - 16 * (lane >> 4) + 8 * (lane & 3), bt = kb + CS * (wh & 3) - 8 * (lane >> 4) + 8 * (lane & 3);
    u32 ta = kb + lane * 4, fa = kb + j * TROW + hh * 16;
    asm volatile("" : "+v"(CR.bands[0]), "+v"(CR.bands[1]), "+v"(CR.bands[2]), "+v"(CR.bands[3]), "+v"(CR.bands[4]), "+v"(a0), "+v"(a1), "+v"(tA), "+v"(tB),
                 "+v"(sa), "+v"(zA), "+v"(cA), "+v"(pA), "+v"(cA2), "+v"(m1), "+v"(m0), "+v"(bn), "+v"(bt), "+v"(ta), "+v"(fa));
    STAMP(7);
    if (wid >= 2) {
      __builtin_amdgcn_s_sleep(SLP_W);
      const u32 row = mm * 16 + (lane >> 2);
      const u32 vo = row * 3584 + ((xc * 448) & ~127) + (lane & 3) * 128;
      dma4nt(vo, wid == 2 ? wg : wu, M_DUM);
    } else {
      u2v tg; u32 spins = 0;
      do { tg = lds_ldv<u2v>(M_TAG); } while (tg.y == 0 && ++spins < SPIN_CAP);
      STAMP(8);
      asm volatile("s_waitcnt vmcnt(7)" : "+v"(xt0), "+v"(xt1), "+v"(xt2), "+v"(xt3), "+v"(xt4), "+v"(xt5), "+v"(xt6), "+v"(xt7) :: "memory");
      STAMP(9);
      const u2v xt[8] = {xt0, xt1, xt2, xt3, xt4, xt5, xt6, xt7};
      u32 c = tg.x;
#pragma unroll
      for (int i = 0; i < 8; i++) c += xt[i].x + xt[i].y;
      const __amdgpu_buffer_rsrc_t rs = __builtin_amdgcn_make_buffer_rsrc(ws + W_XB + (R * 8 + S) * XBS, 0, XBS, 0x00020000);
      const u32 o = (8 * wid + (lane >> 3)) * 512 + (lane & 7) * 16;
#pragma unroll
      for (int i = 0; i < 4; i++)
        __builtin_amdgcn_raw_buffer_store_b128((u4v){xt[2 * i].x, xt[2 * i].y, xt[2 * i + 1].x, xt[2 * i + 1].y}, rs, o + i * 128, 0, 16);
      __builtin_amdgcn_raw_buffer_store_b32(c, rs, 8192 + wid * 256 + lane * 4, 0, 16);
    }
    asm volatile("s_cmp_lt_u32 %7, 2\n\ts_cbranch_scc1 1f\n\ts_waitcnt vmcnt(3)\n1:\n\ts_waitcnt vmcnt(5)"
                 : "+v"(p_ar), "+v"(p_ai), "+v"(p_br), "+v"(p_bi), "+v"(p_cr), "+v"(p_ci), "+v"(p_dt) : "s"(wid) : "memory", "scc");
    STAMP(1);

    const float dt = rbf(__builtin_amdgcn_exp2f(bl(p_dt) * 1.44269504f));
    f16v kd = {};
#pragma unroll 1
    for (int h = 0; h < 2; h++) {
      const u32 sh = 16 - 16 * h;
      const float a2 = dt * bh(p_ar << sh) * 1.44269504f, rv = dt * bh(p_ai << sh) * 0.159154943f;
      const f2 Bc = {dt * bh(p_br << sh), dt * bh(p_bi << sh)}, Cc = {bh(p_cr << sh), bh(p_ci << sh)};
      const f2 G = cmul(Cc, Bc, rot(Bc));
      const f2 w1 = cexp(a2, rv, 1.f), w256 = cexp(a2, rv, 256.f);
      f2 v8 = cexp(a2, -rv, 8.f);
      const f2 w1r = rot(w1), w256r = rot(w256);
      const f2 w2 = cmul(w1, w1, w1r), w2r = rot(w2), w4 = cmul(w2, w2, w2r), w4r = rot(w4);
      const f2 v16 = cmul(v8, v8, rot(v8)), v16r = rot(v16);
#define TW(r, v) lds_st<u32>(ta + (r) * TROW, pk2((v).x, (v).y))
      f2 fz = G, ge = v16, go = v8, fh = cmul(G, w256, w256r);
      TW(0, fz); TW(21, ((f2){1.f, 0.f})); TW(22, go); TW(23, ge); TW(8, fh);
      f2 t2 = cmul(fh, w2, w2r); TW(9, t2); t2 = cmul(t2, w2, w2r); TW(10, t2); t2 = cmul(t2, w2, w2r); TW(11, t2);
#pragma unroll
      for (int i = 1; i < 16; i++) {
        go = cmul(go, v16, v16r); TW(22 + 2 * i, go);
        if (i < 15) { ge = cmul(ge, v16, v16r); TW(23 + 2 * i, ge); }
        if (i < 8) { fz = cmul(fz, w1, w1r); TW(i, fz); }
        else {
          fh = cmul(fh, w256, w256r);
          if (i < 10) { TW(12 + (i - 8), fh); f2 t4 = cmul(fh, w4, w4r); TW(14 + (i - 8), t4); }
          else if (i < 15) TW(16 + (i - 10), fh);
        }
      }
      bf8 tf[16];
#pragma unroll
      for (int s = 0; s < 8; s++) { tf[2 * s] = lds_ld<bf8>(fa + 32 * s); tf[2 * s + 1] = lds_ld<bf8>(fa + 21 * TROW + 32 * s); }
      __builtin_amdgcn_sched_barrier(0);
#pragma unroll
      for (int s = 0; s < 8; s++) kd = __builtin_amdgcn_mfma_f32_32x32x16_bf16(tf[2 * s], tf[2 * s + 1], kd, 0, 0, 0);
    }
#pragma unroll
    for (int q = 0; q < 4; q++) lds_st<u4v>(zA + q * CS, (u4v){0, 0, 0, 0});
    STAMP(2);
    {
      lds_st<u2v>(a0, (u2v){pk2(kd[0], kd[1]), pk2(kd[2], kd[3])});
      const float en = DPPF(kd[4], 0x130, 0xf);
      const u32 e00 = pk2(kd[4], kd[4]);
      lds_st<u4v>(a1, (u4v){e00 & (m0 | m1), (pk2(kd[5], kd[6]) & m0) | (e00 & m1 & 0xffffu), pk2(kd[6], kd[6]) & m0, pk2(kd[7], en) & m0});
      const u4v c = lds_ld<u4v>(cA), c2 = lds_ld<u4v>(cA2);
      const u2v p = lds_ld<u2v>(pA), p2 = lds_ld<u2v>(cA2 - 8);
      const u32 v01 = hh ? pk2(kd[4], kd[5]) : pk2(kd[8], kd[9]), v23 = hh ? pk2(kd[6], kd[7]) : pk2(kd[10], kd[11]);
#pragma unroll
      for (int q = 0; q < 4; q++) {
        lds_st<u16>(tA + q * (CS + 2), (u16)v01); lds_st<u16>(tA + q * (CS + 2) + 32, (u16)(v01 >> 16));
        lds_st<u16>(tB + q * (CS + 2), (u16)v23); lds_st<u16>(tB + q * (CS + 2) + 32, (u16)(v23 >> 16));
      }
      const u32 k8 = pk2(kd[8], kd[8]);
      const u32 al = rl(k8, 0), k2048 = rl(k8, 32);
      lds_st<u16>(sa, (u16)(lane < 8 ? 0u : lane < 16 ? al : k2048));
      lds_st<u32>(M_AL + wid * 4, al << 16);
#define SHIFTS(c, p, A) do { \
      const u32 s0 = __builtin_amdgcn_alignbit(p.y, p.x, 16), s1 = __builtin_amdgcn_alignbit(c.x, p.y, 16), s2 = __builtin_amdgcn_alignbit(c.y, c.x, 16), \
                s3 = __builtin_amdgcn_alignbit(c.z, c.y, 16), s4 = __builtin_amdgcn_alignbit(c.w, c.z, 16); \
      lds_st<u4v>(A + CS, (u4v){s1, s2, s3, s4}); lds_st<u4v>(A + 2 * CS, (u4v){p.y, c.x, c.y, c.z}); lds_st<u4v>(A + 3 * CS, (u4v){s0, s1, s2, s3}); } while (0)
      SHIFTS(c, p, cA);
      SHIFTS(c2, p2, cA2);
    }
    MEMF();
#pragma unroll
    for (int a = 2; a <= 4; a++) {
      CR.T[2 * (a - 2)] = cat(trd(bt + O_TA + 32 * a - 32), trd(bt + O_TB + 32 * a - 32));
      CR.T[2 * (a - 2) + 1] = cat(trd(bt + O_TC + 32 * a - 32), trd(bt + O_TD + 32 * a - 32));
    }
#pragma unroll
    for (int a = 4; a <= 7; a++) CR.T[6 + a - 4] = cat(trd(bt + O_TE + 32 * a - 96), trd(bt + O_TF + 32 * a - 96));
#pragma unroll
    for (int m = 0; m < 17; m++) CR.An[m] = cat(trd(bn + 64 * m + 8), trd(bn + 64 * m));
    u32 ub = UB + wid * UCH + 32 + 32 * (lane & 15) + 16 * (lane >> 4);
    u32 ya = kb + O_Y + 64 * (lane & 15) + 16 * (lane >> 4), fl = M_YF + wid * 4;
    asm volatile("" : "+v"(ub), "+v"(ya), "+v"(fl));
    STAMP(3);
    asm volatile("s_waitcnt lgkmcnt(0)\n\ts_barrier" ::: "memory");
    STAMP(4);
    if (hf == 0) conv<0>(ub, ya, fl, CR); else conv<1>(ub, ya, fl, CR);
    STAMP(5);
    asm volatile("s_waitcnt vmcnt(0) lgkmcnt(0)" ::: "memory");
    STAMP_FLUSH();
    return;
  }

  const int cw = wid - 4;
  const int sp = xc * 32 + mm;
  u32 ep = 0;
  if (cw == 0) ep = __hip_atomic_load((GAS u32*)(ws + W_EP), __ATOMIC_RELAXED, __HIP_MEMORY_SCOPE_AGENT);
  const u4v xr0 = __builtin_nontemporal_load((const GAS u4v*)(x + (size_t)(8 * sp + 2 * cw) * 512) + lane);
  const u4v xr1 = __builtin_nontemporal_load((const GAS u4v*)(x + (size_t)(8 * sp + 2 * cw + 1) * 512) + lane);
  const u4v wv = ((const GAS u4v*)wr)[lane];
  const u2v dk = *(const GAS u2v*)(dsk + 4 * cg);
  if (cw == 3) lds_st<u32>(MB + lane * 4, 0u);
  asm volatile("s_waitcnt lgkmcnt(0)\n\ts_barrier" ::: "memory");
  u32 tag;
  if (cw == 0) {
    tag = __builtin_amdgcn_readfirstlane(ep) + 1u;
    if (lane == 0) lds_stv<u2v>(M_TAG, (u2v){tag, 1u});
  }
  {
    const bf2 ones = __builtin_bit_cast(bf2, 0x3F803F80u);
    float s0 = 0, q0 = 0, d0 = 0, s1 = 0, q1 = 0, d1 = 0, sw = 0;
#pragma unroll
    for (int i = 0; i < 4; i++) {
      const bf2 a = asbf2(xr0[i]), b = asbf2(xr1[i]), c = asbf2(wv[i]);
      s0 = __builtin_amdgcn_fdot2_f32_bf16(a, ones, s0, false); q0 = __builtin_amdgcn_fdot2_f32_bf16(a, a, q0, false); d0 = __builtin_amdgcn_fdot2_f32_bf16(a, c, d0, false);
      s1 = __builtin_amdgcn_fdot2_f32_bf16(b, ones, s1, false); q1 = __builtin_amdgcn_fdot2_f32_bf16(b, b, q1, false); d1 = __builtin_amdgcn_fdot2_f32_bf16(b, c, d1, false);
      sw = __builtin_amdgcn_fdot2_f32_bf16(c, ones, sw, false);
    }
    auto sw32 = [](float a, float b) { auto r = __builtin_amdgcn_permlane32_swap(__float_as_uint(a), __float_as_uint(b), false, false); return __uint_as_float(r[0]) + __uint_as_float(r[1]); };
    float s = sw32(s0, s1), q = sw32(q0, q1), dd = sw32(d0, d1); sw = sw32(sw, sw);
#define RED(v) v += DPPF(v, 0x128, 0xf);
    s += DPPF(s, 0x128, 0xf); q += DPPF(q, 0x128, 0xf); dd += DPPF(dd, 0x128, 0xf); sw += DPPF(sw, 0x128, 0xf);
    s += DPPF(s, 0x124, 0xf); q += DPPF(q, 0x124, 0xf); dd += DPPF(dd, 0x124, 0xf); sw += DPPF(sw, 0x124, 0xf);
    s += DPPF(s, 0x122, 0xf); q += DPPF(q, 0x122, 0xf); dd += DPPF(dd, 0x122, 0xf); sw += DPPF(sw, 0x122, 0xf);
    s += DPPF(s, 0x121, 0xf); q += DPPF(q, 0x121, 0xf); dd += DPPF(dd, 0x121, 0xf); sw += DPPF(sw, 0x121, 0xf);
    s += DPPF(s, 0x142, 0xa); q += DPPF(q, 0x142, 0xa); dd += DPPF(dd, 0x142, 0xa); sw += DPPF(sw, 0x142, 0xa);
    const float mean = s * (1.f / 512.f);
    const float var = fmaxf(q * (1.f / 512.f) - mean * mean, 0.f);
    const float mb = rbf(mean), sb = rbf(__builtin_amdgcn_sqrtf(rbf(rbf(var) + 1e-5f)));
    const float r = (dd - mb * sw) * __builtin_amdgcn_rcpf(sb);
    const u32 ms = pk2(mb, sb), rb = pk2(r, r) & 0xffffu;
    const u32 ms0 = rl(ms, 31), ms1 = rl(ms, 63), rr = rl(rb, 31) | (rl(rb, 63) << 16);
    if (cw != 0) {
      u2v tg; u32 spins = 0;
      do { tg = lds_ldv<u2v>(M_TAG); } while (tg.y == 0 && ++spins < SPIN_CAP);
      tag = __builtin_amdgcn_readfirstlane(tg.x);
    }
    const __amdgpu_buffer_rsrc_t rs = __builtin_amdgcn_make_buffer_rsrc(ws + W_ST, 0, 16384, 0x00020000);
    if (lane == 0) __builtin_amdgcn_raw_buffer_store_b128((u4v){ms0, ms1, rr, ms0 + ms1 + rr + tag + tag - 1u}, rs, cw * 4096 + sp * 16, 0, 16);
  }
  STAMP(1);
  const u32 chs = tag + tag - 1u;
  u4v su0, su1, su2, su3, xu0, xu1, xu2, xu3; u32 xk;
  const u32 so = W_ST + (64 * cw + lane) * 16;
  const u32 xb = W_XB + ((8 * cw + (lane >> 3)) * 8 + xc) * XBS;
  const u32 xo = xb + (mm >> 1) * 512 + (lane & 7) * 16, xko = xb + 8192 + (mm >> 1) * 32 + (lane & 7) * 4;
  const char* ws1 = ws + 4096; const char* ws2 = ws + 8192; const char* ws3 = ws + 12288;
#define LOAD_S() do { GLD4_SC1(su0, so, ws, 0); GLD4_SC1(su1, so, ws1, 0); GLD4_SC1(su2, so, ws2, 0); GLD4_SC1(su3, so, ws3, 0); } while (0)
#define LOAD_X() do { GLD4_SC1(xu0, xo, ws, 0); GLD4_SC1(xu1, xo, ws, 128); GLD4_SC1(xu2, xo, ws, 256); GLD4_SC1(xu3, xo, ws, 384); GLD1_SC1(xk, xko, ws, 0); } while (0)
#define OK_S() ((((su0.x + su0.y + su0.z + chs) ^ su0.w) | ((su1.x + su1.y + su1.z + chs) ^ su1.w) | ((su2.x + su2.y + su2.z + chs) ^ su2.w) | ((su3.x + su3.y + su3.z + chs) ^ su3.w)) == 0)
#define SUM4(v) (v.x + v.y + v.z + v.w)
#define OK_X() (SUM4(xu0) + SUM4(xu1) + SUM4(xu2) + SUM4(xu3) + tag == xk)
  __builtin_amdgcn_s_sleep(SLP_S);
  LOAD_S();
  __builtin_amdgcn_s_sleep(SLP_X);
  LOAD_X();
  const u32 fb = FB + cw * 12288;
  const int fp = xc * 14 + mm;
#define PULL() do { \
    const u32 go = (128 * cw + (lane >> 1)) * 3584 + fp * 32 + (lane & 1) * 16; \
    constexpr u32 GS = 32 * 3584 - 1024; \
    dma16x4(go, go + GS, go + 2 * GS, go + 3 * GS, wg, fb); \
    dma16x4(go, go + GS, go + 2 * GS, go + 3 * GS, wu, fb + 4096); \
    const u32 dof = (16 * fp + (lane >> 4)) * 1024 + 256 * cw + (lane & 15) * 16; \
    dma16x4(dof, dof + 3072, dof + 6144, dof + 9216, wd, fb + 8192); } while (0)
  asm volatile("s_waitcnt vmcnt(5)" : "+v"(su0), "+v"(su1), "+v"(su2), "+v"(su3) :: "memory");
  STAMP(2);
  for (u32 spins = 0; !__all(OK_S()) && spins < SPIN_CAP; spins++) {
    asm volatile("s_sleep 2\n\tglobal_load_dwordx4 %0, %4, %5 sc1\n\tglobal_load_dwordx4 %1, %4, %6 sc1\n\tglobal_load_dwordx4 %2, %4, %7 sc1\n\t"
                 "global_load_dwordx4 %3, %4, %8 sc1\n\ts_waitcnt vmcnt(0)"
                 : "=&v"(su0), "=&v"(su1), "=&v"(su2), "=&v"(su3) : "v"(so), "s"(ws), "s"(ws1), "s"(ws2), "s"(ws3) : "memory");
  }
  STAMP(3);
  u32 k1, k2;
  {
    const u32 ib = 0xffffu - (512 * cw + 8 * lane);
    auto key = [&](u32 b, int i) { u32 o = b ^ ((u32)((int)b >> 31) | 0x80000000u); return (o & 0xffff0000u) | (ib - i); };
    k1 = key(su0.z << 16, 0); k2 = 0;
    auto ins = [&](u32 k) { k2 = umax(k2, umin(k1, k)); k1 = umax(k1, k); };
    ins(key(su0.z & 0xffff0000u, 1)); ins(key(su1.z << 16, 2)); ins(key(su1.z & 0xffff0000u, 3)); ins(key(su2.z << 16, 4));
    ins(key(su2.z & 0xffff0000u, 5)); ins(key(su3.z << 16, 6)); ins(key(su3.z & 0xffff0000u, 7));
    const u32 K1 = wave_umax(k1), K2 = wave_umax(k1 == K1 ? k2 : k1);
    if (lane == 0) lds_stv<u2v>(M_TOP + cw * 8, (u2v){K1, K2});
    u4v ta, tb; u32 spins = 0;
    do { ta = lds_ldv<u4v>(M_TOP); tb = lds_ldv<u4v>(M_TOP + 16); } while ((ta.x == 0 || ta.z == 0 || tb.x == 0 || tb.z == 0) && ++spins < SPIN_CAP);
    const u32 a1 = umax(ta.x, ta.z), a2 = umax(umin(ta.x, ta.z), umax(ta.y, ta.w)), b1 = umax(tb.x, tb.z), b2 = umax(umin(tb.x, tb.z), umax(tb.y, tb.w));
    k1 = __builtin_amdgcn_readfirstlane(umax(a1, b1)); k2 = __builtin_amdgcn_readfirstlane(umax(umin(a1, b1), umax(a2, b2)));
  }
  const int t0 = (0xffffu - (k1 & 0xffffu)) & 2047, t1 = (0xffffu - (k2 & 0xffffu)) & 2047;
  u4v xs0, xs1, xs2, xs3;
  if (prod) {
    const u32 xso = ((lane & 1) ? t1 : t0) * 1024 + 256 * cw + 16 * (lane >> 4);
    GLD4(xs0, xso, x, 0); GLD4(xs1, xso, x, 64); GLD4(xs2, xso, x, 128); GLD4(xs3, xso, x, 192);
  }
  if (w == 0 && cw == 0 && lane == 0) __hip_atomic_store((GAS u32*)(ws + W_EP), tag, __ATOMIC_RELAXED, __HIP_MEMORY_SCOPE_AGENT);
  if ((t0 >> 3) == 64 * cw + lane) { lds_st<u4v>(M_SEL, (u4v){su0.x, su0.y, su1.x, su1.y}); lds_st<u4v>(M_SEL + 16, (u4v){su2.x, su2.y, su3.x, su3.y}); }
  if ((t1 >> 3) == 64 * cw + lane) { lds_st<u4v>(M_SEL + 32, (u4v){su0.x, su0.y, su1.x, su1.y}); lds_st<u4v>(M_SEL + 48, (u4v){su2.x, su2.y, su3.x, su3.y}); }
  float na[8], nb[8];
  {
    const u32 msv[8] = {su0.x, su0.y, su1.x, su1.y, su2.x, su2.y, su3.x, su3.y};
#pragma unroll
    for (int i = 0; i < 8; i++) { na[i] = __builtin_amdgcn_rcpf(bh(msv[i])); nb[i] = -bl(msv[i]) * na[i]; }
  }
  asm volatile("" : "+v"(na[0]), "+v"(na[1]), "+v"(na[2]), "+v"(na[3]), "+v"(na[4]), "+v"(na[5]), "+v"(na[6]), "+v"(na[7]),
               "+v"(nb[0]), "+v"(nb[1]), "+v"(nb[2]), "+v"(nb[3]), "+v"(nb[4]), "+v"(nb[5]), "+v"(nb[6]), "+v"(nb[7]));
  STAMP(4);
#define XW "4"
  asm volatile("s_cmp_lt_u32 %5, 111\n\ts_cbranch_scc1 1f\n\ts_waitcnt vmcnt(0)\n1:\n\ts_waitcnt vmcnt(" XW ")"
               : "+v"(xu0), "+v"(xu1), "+v"(xu2), "+v"(xu3), "+v"(xk) : "s"(w - 1) : "memory", "scc");
  for (u32 spins = 0; !__all(OK_X()) && spins < SPIN_CAP; spins++) {
    asm volatile("s_sleep 1\n\tglobal_load_dwordx4 %0, %5, %7 sc1\n\tglobal_load_dwordx4 %1, %5, %7 offset:128 sc1\n\tglobal_load_dwordx4 %2, %5, %7 offset:256 sc1\n\t"
                 "global_load_dwordx4 %3, %5, %7 offset:384 sc1\n\tglobal_load_dword %4, %6, %7 sc1\n\ts_waitcnt vmcnt(0)"
                 : "=&v"(xu0), "=&v"(xu1), "=&v"(xu2), "=&v"(xu3), "=&v"(xk) : "v"(xo), "v"(xko), "s"(ws) : "memory");
  }
  STAMP(5);
  u32 xa[8] = {xu0.x, xu0.z, xu1.x, xu1.z, xu2.x, xu2.z, xu3.x, xu3.z};
  u32 xcv[8] = {xu0.y, xu0.w, xu1.y, xu1.w, xu2.y, xu2.w, xu3.y, xu3.w};
  {
    u4v c0, c1, c2, c3;
#pragma unroll
    for (int i = 0; i < 4; i++) {
      const int r0 = 2 * i, r1 = 2 * i + 1;
      const f2 p0 = __builtin_elementwise_fma((f2){bl(xa[r0]), bh(xa[r0])}, (f2){na[r0], na[r0]}, (f2){nb[r0], nb[r0]});
      const f2 p1 = __builtin_elementwise_fma((f2){bl(xa[r1]), bh(xa[r1])}, (f2){na[r1], na[r1]}, (f2){nb[r1], nb[r1]});
      const f2 p2 = __builtin_elementwise_fma((f2){bl(xcv[r0]), bh(xcv[r0])}, (f2){na[r0], na[r0]}, (f2){nb[r0], nb[r0]});
      const f2 p3 = __builtin_elementwise_fma((f2){bl(xcv[r1]), bh(xcv[r1])}, (f2){na[r1], na[r1]}, (f2){nb[r1], nb[r1]});
      c0[i] = pk2(p0.x, p1.x); c1[i] = pk2(p0.y, p1.y); c2[i] = pk2(p2.x, p3.x); c3[i] = pk2(p2.y, p3.y);
    }
    const u32 ua = UB + 512 + (64 * cw + lane) * 16;
    lds_st<u4v>(ua, c0); lds_st<u4v>(ua + UCH, c1); lds_st<u4v>(ua + 2 * UCH, c2); lds_st<u4v>(ua + 3 * UCH, c3);
  }
  asm volatile("s_waitcnt lgkmcnt(0)\n\ts_barrier" ::: "memory");
  STAMP(6);

  if (prod) {
    PULL();
    const u32 msel = lds_ld<u32>(M_SEL + (lane & 1) * 32 + (((lane & 1) ? t1 : t0) & 7) * 4);
    const float a = __builtin_amdgcn_rcpf(bh(msel)), b = -bl(msel) * a;
    asm volatile("s_waitcnt vmcnt(0)" : "+v"(xs0), "+v"(xs1), "+v"(xs2), "+v"(xs3) :: "memory");
    STAMP(7);
    auto nrm = [&](u4v v) { u4v r;
#pragma unroll
      for (int i = 0; i < 4; i++) r[i] = pk2(fmaf(bl(v[i]), a, b), fmaf(bh(v[i]), a, b));
      return __builtin_bit_cast(bf8, r); };
    const bf8 xf[4] = {nrm(xs0), nrm(xs1), nrm(xs2), nrm(xs3)};
    const int g = lane >> 4, q = (lane >> 2) & 3, p = lane & 3;
    const u32 ga = fb + (8 * g + q) * 32 + 8 * p;
    f4 ag = {0, 0, 0, 0}, au = {0, 0, 0, 0};
#pragma unroll
    for (int s = 0; s < 4; s++) {
      ag = mma(cat(trd(ga + s * 1024), trd(ga + s * 1024 + 128)), xf[s], ag);
      au = mma(cat(trd(ga + 4096 + s * 1024), trd(ga + 4096 + s * 1024 + 128)), xf[s], au);
    }
    if ((lane & 15) < 2) {
      lds_st<f4>(M_PART + cw * 256 + (lane & 1) * 64 + g * 16, ag);
      lds_st<f4>(M_PART + cw * 256 + 128 + (lane & 1) * 64 + g * 16, au);
    }
    MEMF();
    if (lane == 0) lds_stv<u32>(M_FF + cw * 4, 1u);
    u4v ff; u32 spins = 0;
    do { ff = lds_ldv<u4v>(M_FF); } while ((ff.x & ff.y & ff.z & ff.w) == 0 && ++spins < SPIN_CAP);
    MEMF();
    f4 sg = {0, 0, 0, 0}, su = {0, 0, 0, 0};
#pragma unroll
    for (int v = 0; v < 4; v++) {
      sg += lds_ld<f4>(M_PART + v * 256 + (lane & 1) * 64 + g * 16);
      su += lds_ld<f4>(M_PART + v * 256 + 128 + (lane & 1) * 64 + g * 16);
    }
    float hv[4];
#pragma unroll
    for (int i = 0; i < 4; i++) { const float gg = rbf(sg[i]); hv[i] = rbf(gg * sigm(gg)) * rbf(su[i]); }
    const s4v hb = __builtin_bit_cast(s4v, ((u2v){pk2(hv[0], hv[1]), pk2(hv[2], hv[3])}));
    const u32 da = fb + 8192 + g * 1024 + q * 256 + 8 * p;
    const __amdgpu_buffer_rsrc_t rs = __builtin_amdgcn_make_buffer_rsrc(ws + W_FB + fp * 8192, 0, 8192, 0x00020000);
    const u32 fo = (32 * cw + g) * 64 + (lane & 1) * 32 + (lane & 2) * 8;
#pragma unroll
    for (int t = 0; t < 8; t++) {
      const f4 y = __builtin_amdgcn_mfma_f32_16x16x16bf16_1k(__builtin_bit_cast(s4v, trd(da + 32 * t)), hb, (f4){0, 0, 0, 0}, 0, 0, 0);
      const u4v o = {__float_as_uint((lane & 2) ? y[2] : y[0]), tag, __float_as_uint((lane & 2) ? y[3] : y[1]), tag};
      if ((lane & 12) == 0) __builtin_amdgcn_raw_buffer_store_b128(o, rs, fo + t * 256, 0, 16);
    }
    STAMP(8);
  }

  const int up = (cw & 1) ^ hf;
  const int tile = 2 * cw + up;
  const int tl = 8 * (lane & 31) + 4 * (lane >> 5), tr0 = 256 * tile + tl;
  float xv[4][4], uv[4][4], pre[4][4], gt[4][4];
#pragma unroll
  for (int r = 0; r < 4; r++) {
    auto s0 = __builtin_amdgcn_permlane32_swap(xa[r], xa[r + 4], false, false);
    auto s1 = __builtin_amdgcn_permlane32_swap(xcv[r], xcv[r + 4], false, false);
    const u32 e0 = up ? s0[1] : s0[0], e1 = up ? s1[1] : s1[0];
    xv[0][r] = bl(e0); xv[1][r] = bh(e0); xv[2][r] = bl(e1); xv[3][r] = bh(e1);
  }
  {
    const f4 alv = lds_ld<f4>(M_AL);
    const float dv[4] = {bl(dk.x), bh(dk.x), bl(dk.y), bh(dk.y)};
    const u32 ua = UB + 512 + tr0 * 2;
    const u32 ca = tile >= 4 ? ua - 2064 : UB;
#pragma unroll
    for (int c = 0; c < 4; c++) {
      const u2v a = lds_ld<u2v>(ua + c * UCH), p = lds_ld<u2v>(ca + c * UCH), n = lds_ld<u2v>(ca + c * UCH + 8);
      uv[c][0] = bl(a.x); uv[c][1] = bh(a.x); uv[c][2] = bl(a.y); uv[c][3] = bh(a.y);
      pre[c][0] = uv[c][0] * dv[c] - alv[c] * bh(p.y); pre[c][1] = uv[c][1] * dv[c] - alv[c] * bl(n.x);
      pre[c][2] = uv[c][2] * dv[c] - alv[c] * bh(n.x); pre[c][3] = uv[c][3] * dv[c] - alv[c] * bl(n.y);
#pragma unroll
      for (int r = 0; r < 4; r++) gt[c][r] = uv[c][r] * sigm(uv[c][r]);
    }
  }
  STAMP(9);
  {
    u4v yf; u32 spins = 0;
    do { __builtin_amdgcn_s_sleep(1); yf = lds_ldv<u4v>(M_YF + cw * 16); } while (umin(umin(yf.x, yf.y), umin(yf.z, yf.w)) == 0 && ++spins < SPIN_CAP);
  }
  MEMF();
  STAMP(10);
#pragma unroll
  for (int c = 0; c < 4; c++) {
    const f4 y = lds_ld<f4>(c * KREG + O_Y + cw * 1024 + tl * 4);
#pragma unroll
    for (int r = 0; r < 4; r++) pre[c][r] += y[r];
  }
  const bool late = (tr0 >> 2) == (t0 >> 2) || (tr0 >> 2) == (t1 >> 2);
  char* op = (char*)out + (size_t)tr0 * 1024 + cg * 8;
  if (!late) {
#pragma unroll
    for (int r = 0; r < 4; r++)
      *(GAS u2v*)(op + r * 1024) = (u2v){pk2(fmaf(pre[0][r], gt[0][r], xv[0][r]), fmaf(pre[1][r], gt[1][r], xv[1][r])),
                                         pk2(fmaf(pre[2][r], gt[2][r], xv[2][r]), fmaf(pre[3][r], gt[3][r], xv[3][r]))};
  }
  STAMP(11);
  if ((t0 >> 8) == tile || (t1 >> 8) == tile) {
#pragma unroll 1
    for (int k = 0; k < 2; k++) {
      const int tk = k ? t1 : t0; const u32 kk = k ? k2 : k1;
      if ((tk >> 8) != tile) continue;
      const u32 fo = W_FB + (lane >> 1) * 8192 + cg * 64 + k * 32 + (lane & 1) * 16;
      const u32 fo3 = lane < 32 ? fo + 96 * 8192 : fo;
      u4v g0, g1, g2, g3;
      for (u32 spins = 0; spins < SPIN_CAP; spins++) {
        asm volatile("global_load_dwordx4 %0, %4, %8 sc1\n\tglobal_load_dwordx4 %1, %5, %8 sc1\n\tglobal_load_dwordx4 %2, %6, %8 sc1\n\t"
                     "global_load_dwordx4 %3, %7, %8 sc1\n\ts_waitcnt vmcnt(0)"
                     : "=&v"(g0), "=&v"(g1), "=&v"(g2), "=&v"(g3) : "v"(fo), "v"(fo + 32 * 8192), "v"(fo + 64 * 8192), "v"(fo3), "s"(ws) : "memory");
        if (__all(((g0.y ^ tag) | (g0.w ^ tag) | (g1.y ^ tag) | (g1.w ^ tag) | (g2.y ^ tag) | (g2.w ^ tag) | (g3.y ^ tag) | (g3.w ^ tag)) == 0)) break;
        __builtin_amdgcn_s_sleep(1);
      }
      float v0 = __uint_as_float(g0.x) + __uint_as_float(g1.x) + __uint_as_float(g2.x) + (lane < 32 ? __uint_as_float(g3.x) : 0.f);
      float v1 = __uint_as_float(g0.z) + __uint_as_float(g1.z) + __uint_as_float(g2.z) + (lane < 32 ? __uint_as_float(g3.z) : 0.f);
      v0 += DPPF(v0, 0x128, 0xf); v1 += DPPF(v1, 0x128, 0xf); v0 += DPPF(v0, 0x124, 0xf); v1 += DPPF(v1, 0x124, 0xf);
      v0 += DPPF(v0, 0x122, 0xf); v1 += DPPF(v1, 0x122, 0xf);
      float yv[4];
      {
        const u32 a = __float_as_uint(v0), b = __float_as_uint(v1);
        yv[0] = (__uint_as_float(rl(a, 0)) + __uint_as_float(rl(a, 16))) + (__uint_as_float(rl(a, 32)) + __uint_as_float(rl(a, 48)));
        yv[1] = (__uint_as_float(rl(b, 0)) + __uint_as_float(rl(b, 16))) + (__uint_as_float(rl(b, 32)) + __uint_as_float(rl(b, 48)));
        yv[2] = (__uint_as_float(rl(a, 1)) + __uint_as_float(rl(a, 17))) + (__uint_as_float(rl(a, 33)) + __uint_as_float(rl(a, 49)));
        yv[3] = (__uint_as_float(rl(b, 1)) + __uint_as_float(rl(b, 17))) + (__uint_as_float(rl(b, 33)) + __uint_as_float(rl(b, 49)));
      }
      const u32 ob = kk & 0xffff0000u;
      const float rs = rbf(sigm(__uint_as_float((ob & 0x80000000u) ? (ob ^ 0x80000000u) : (~ob & 0xffff0000u))));
#pragma unroll
      for (int c = 0; c < 4; c++) {
        const float upd = rbf(rbf(yv[c]) * rs);
#pragma unroll
        for (int r = 0; r < 4; r++) {
          const float b = rbf(uv[c][r] + upd);
          gt[c][r] = (tr0 + r == tk) ? b * sigm(b) : gt[c][r];
        }
      }
    }
    if (late) {
#pragma unroll
      for (int r = 0; r < 4; r++)
        *(GAS u2v*)(op + r * 1024) = (u2v){pk2(fmaf(pre[0][r], gt[0][r], xv[0][r]), fmaf(pre[1][r], gt[1][r], xv[1][r])),
                                           pk2(fmaf(pre[2][r], gt[2][r], xv[2][r]), fmaf(pre[3][r], gt[3][r], xv[3][r]))};
    }
    STAMP(12);
  }
}

extern "C" void kernel_launch(void* const* d_in, const int* in_sizes, int n_in, void* d_out, int out_size,
                              void* d_ws, size_t ws_size, hipStream_t stream) {
  (void)hipFuncSetAttribute((const void*)s4_moe_kernel, hipFuncAttributeMaxDynamicSharedMemorySize, LDS_BYTES);
  s4_moe_kernel<<<256, 512, LDS_BYTES, stream>>>(
      (const u16*)d_in[0], (const u16*)d_in[1], (const u16*)d_in[2], (const u16*)d_in[3], (const u16*)d_in[4],
      (const u16*)d_in[5], (const u16*)d_in[6], (const u16*)d_in[7], (const u16*)d_in[8], (const u16*)d_in[10],
      (const u16*)d_in[11], (const u16*)d_in[12], (const u16*)d_in[13], (u16*)d_out, (char*)d_ws, 0ull);
}
```

```cpp
#define KMODE 0
#define PROBE 0
#define PW 200
#define PWAVE 4
#define PULL_LATE
#include <hip/hip_runtime.h>
#include <hip/hip_bf16.h>
#include <stdint.h>

typedef unsigned short u16;
typedef unsigned int u32;
typedef unsigned long long u64;
typedef float f2 __attribute__((ext_vector_type(2)));
typedef float f4 __attribute__((ext_vector_type(4)));
typedef float f16v __attribute__((ext_vector_type(16)));
typedef u32 u2v __attribute__((ext_vector_type(2)));
typedef u32 u4v __attribute__((ext_vector_type(4)));
typedef __bf16 bf2 __attribute__((ext_vector_type(2)));
typedef __bf16 bf4 __attribute__((ext_vector_type(4)));
typedef __bf16 bf8 __attribute__((ext_vector_type(8)));
typedef short s4v __attribute__((ext_vector_type(4)));
#define LAS __attribute__((address_space(3)))
#define GAS __attribute__((address_space(1)))
#define DEVI __device__ __forceinline__

#define SLP_S 10
#define SLP_X 4
#define SLP_W 20
#define SPIN_CAP (1u << 18)

constexpr int KREG = 14592;
constexpr int TROW = 272;
constexpr int CS = 2112;
constexpr int O_TA = 1120, O_TB = 1248, O_TC = 1376, O_TD = 1504, O_TE = 1632, O_TF = 1792;
constexpr int O_Y = 8448;
constexpr int UB = 4 * KREG;
constexpr int UCH = 4608;
constexpr int FB = UB + 4 * UCH;
constexpr int MB = FB + 49152;
constexpr int M_TAG = MB;
constexpr int M_TOP = MB + 16;
constexpr int M_YF = MB + 64;
constexpr int M_FF = MB + 128;
constexpr int M_AL = MB + 144;
constexpr int M_SEL = MB + 192;
constexpr int M_PART = MB + 256;
constexpr int M_DUM = MB + 1280;
constexpr int LDS_BYTES = MB + 1536;

constexpr u32 W_EP = 0;
constexpr u32 W_ST = 4096;
constexpr u32 W_XB = 65536;
constexpr u32 XBS = 8704;
constexpr u32 W_FB = 4u << 20;
constexpr u32 W_STAMP = 8u << 20;

#undef STAMPS
#define STAMP(i) do { if ((i) == 0) asm volatile("s_memrealtime %0\n\ts_waitcnt lgkmcnt(0)" : "=s"(pt0) :: "memory"); if ((i) == PROBE) asm volatile("s_memrealtime %0\n\ts_waitcnt lgkmcnt(0)" : "=s"(ptk) :: "memory"); } while (0)
#define STAMP_DECL u64 pt0 = 0, ptk = 0
#define STAMP_FLUSH() do { if (blockIdx.x == PW && wid == PWAVE) { u64 e_ = pt0 + 3000 + 10 * (ptk - pt0); while (__builtin_amdgcn_s_memrealtime() < e_) {} } } while (0)
#define CODE_LINES 64

DEVI float bl(u32 v) { return __uint_as_float(v << 16); }
DEVI float bh(u32 v) { return __uint_as_float(v & 0xffff0000u); }
DEVI u32 pk2(float a, float b) { f2 v = {a, b}; return __builtin_bit_cast(u32, __builtin_convertvector(v, bf2)); }
DEVI bf2 asbf2(u32 v) { return __builtin_bit_cast(bf2, v); }
DEVI float rbf(float a) { return bl(pk2(a, a)); }
DEVI f2 cmul(f2 x, f2 y, f2 yr) { return __builtin_elementwise_fma(x.yy, yr, x.xx * y); }
DEVI f2 rot(f2 y) { return (f2){-y.y, y.x}; }
DEVI f2 cexp(float a2, float rev, float l) {
  float e = __builtin_amdgcn_exp2f(a2 * l), p = __builtin_amdgcn_fractf(rev * l);
  return (f2){e * __builtin_amdgcn_cosf(p), e * __builtin_amdgcn_sinf(p)};
}
DEVI float sigm(float v) { return __builtin_amdgcn_rcpf(1.f + __builtin_amdgcn_exp2f(v * -1.44269504f)); }
template <class T> DEVI T lds_ld(u32 a) { return *(const LAS T*)(uintptr_t)a; }
template <class T> DEVI void lds_st(u32 a, T v) { *(LAS T*)(uintptr_t)a = v; }
template <class T> DEVI T lds_ldv(u32 a) { return *(const volatile LAS T*)(uintptr_t)a; }
template <class T> DEVI void lds_stv(u32 a, T v) { *(volatile LAS T*)(uintptr_t)a = v; }
DEVI bf4 trd(u32 a) { return __builtin_amdgcn_ds_read_tr16_b64_v4bf16((LAS bf4*)(uintptr_t)a); }
DEVI bf8 cat(bf4 a, bf4 b) { return __builtin_shufflevector(a, b, 0, 1, 2, 3, 4, 5, 6, 7); }
DEVI f4 mma(bf8 a, bf8 b, f4 c) { return __builtin_amdgcn_mfma_f32_16x16x32_bf16(a, b, c, 0, 0, 0); }
DEVI u32 rl(u32 v, int l) { return __builtin_amdgcn_readlane(v, l); }
#define DPPF(v, ctl, rm) __uint_as_float(__builtin_amdgcn_update_dpp(0u, __float_as_uint(v), ctl, rm, 0xf, true))
#define DPPU(v, ctl, rm) __builtin_amdgcn_update_dpp(0u, v, ctl, rm, 0xf, true)
DEVI u32 umax(u32 a, u32 b) { return a > b ? a : b; }
DEVI u32 umin(u32 a, u32 b) { return a < b ? a : b; }
DEVI u32 wave_umax(u32 v) {
  v = umax(v, DPPU(v, 0x128, 0xf)); v = umax(v, DPPU(v, 0x124, 0xf)); v = umax(v, DPPU(v, 0x122, 0xf)); v = umax(v, DPPU(v, 0x121, 0xf));
  v = umax(v, DPPU(v, 0x142, 0xa)); v = umax(v, DPPU(v, 0x143, 0xc));
  return rl(v, 63);
}
#define MEMF() asm volatile("" ::: "memory")

#define GLD4_SC1(d, vo, base, imm) asm volatile("global_load_dwordx4 %0, %1, %2 offset:%3 sc1" : "=v"(d) : "v"(vo), "s"(base), "i"(imm) : "memory")
#define GLD1_SC1(d, vo, base, imm) asm volatile("global_load_dword %0, %1, %2 offset:%3 sc1" : "=v"(d) : "v"(vo), "s"(base), "i"(imm) : "memory")
#define GLD2_NT(d, vo, base, imm) asm volatile("global_load_dwordx2 %0, %1, %2 offset:%3 nt" : "=v"(d) : "v"(vo), "s"(base), "i"(imm) : "memory")
#define GLD1_NT(d, vo, base) asm volatile("global_load_dword %0, %1, %2 nt" : "=v"(d) : "v"(vo), "s"(base) : "memory")
#define GLDH(d, vo, base) asm volatile("global_load_ushort %0, %1, %2" : "=v"(d) : "v"(vo), "s"(base) : "memory")
#define GLD4(d, vo, base, imm) asm volatile("global_load_dwordx4 %0, %1, %2 offset:%3" : "=v"(d) : "v"(vo), "s"(base), "i"(imm) : "memory")
DEVI void dma16(u32 vo, const void* base, u32 ldsdst) {
  u32 keep;
  asm volatile("s_mov_b32 %0, m0\n\ts_mov_b32 m0, %3\n\ts_nop 0\n\tglobal_load_lds_dwordx4 %1, %2\n\ts_mov_b32 m0, %0" : "=&s"(keep) : "v"(vo), "s"(base), "s"(ldsdst) : "memory");
}
DEVI void dma16x4(u32 v0, u32 v1, u32 v2, u32 v3, const void* base, u32 ldsdst) {
  u32 keep;
  asm volatile("s_mov_b32 %0, m0\n\ts_mov_b32 m0, %6\n\ts_nop 0\n\tglobal_load_lds_dwordx4 %1, %5\n\tglobal_load_lds_dwordx4 %2, %5 offset:1024\n\t"
               "global_load_lds_dwordx4 %3, %5 offset:2048\n\tglobal_load_lds_dwordx4 %4, %5 offset:3072\n\ts_mov_b32 m0, %0"
               : "=&s"(keep) : "v"(v0), "v"(v1), "v"(v2), "v"(v3), "s"(base), "s"(ldsdst) : "memory");
}
DEVI void dma4nt(u32 vo, const void* base, u32 ldsdst) {
  u32 keep;
  asm volatile("s_mov_b32 %0, m0\n\ts_mov_b32 m0, %3\n\ts_nop 0\n\tglobal_load_lds_dword %1, %2 nt\n\ts_mov_b32 m0, %0" : "=&s"(keep) : "v"(vo), "s"(base), "s"(ldsdst) : "memory");
}
DEVI void dma4(u32 vo, const void* base, u32 ldsdst) {
  u32 keep;
  asm volatile("s_mov_b32 %0, m0\n\ts_mov_b32 m0, %3\n\ts_nop 0\n\tglobal_load_lds_dword %1, %2\n\ts_mov_b32 m0, %0" : "=&s"(keep) : "v"(vo), "s"(base), "s"(ldsdst) : "memory");
}

DEVI bf8 band(int lane, int co, int hw) {
  int lo = (lane & 15) + co - hw - 8 * (lane >> 4);
  u32 m = (u32)(((u64)((1u << (2 * hw + 1)) - 1) << (lo + 24)) >> 24);
  u4v r;
#pragma unroll
  for (int i = 0; i < 4; i++) r[i] = ((m >> (2 * i)) & 1u) * 0x3F80u + ((m >> (2 * i + 1)) & 1u) * 0x3F800000u;
  return __builtin_bit_cast(bf8, r);
}

struct Sched {
  int n, nl, nd;
  int kind[128], p0[128], p1[128], ld[128], tile[128];
  int loff[96];
  int dsrc[40];
  int last[4];
  int lcum[129], ccum[129], yat[4];
};
constexpr int PFD = 8;
constexpr int lswap(int k, int n) { return (k ^ 1) < n ? (k ^ 1) : k; }
constexpr int tile_of(int hf, int i) { return hf == 0 ? (i == 0 ? 0 : i == 1 ? 3 : i == 2 ? 4 : 7) : (i == 0 ? 1 : i == 1 ? 2 : i == 2 ? 5 : 6); }
struct Sched2 { Sched s; int dmap[32]; };
constexpr Sched2 make_sched(int hf) {
  Sched2 r{}; Sched& s = r.s;
  for (int i = 0; i < 4; i++) {
    const int q = tile_of(hf, i), pq = i ? tile_of(hf, i - 1) : -1;
    for (int e = 0; e < 6; e++) {
      const bool n2 = (e <= q - 2) && (e >= q - 4) && !((e <= pq - 2) && (e >= pq - 4));
      const bool n3 = (e <= q - 4) && !(e <= pq - 4);
      if (!(n2 || n3)) continue;
      const int l24 = s.nl++, l16 = s.nl++;
      s.loff[l24] = 64 * (8 * e + 7) - 16; s.loff[l16] = 64 * (8 * e + 7);
      const int bsel[6] = {0, 0, 1, 2, 3, 4}, lsel[6] = {l24, l16, l16, l16, l24, l16};
      const int dm[6] = {4 * e, 4 * e + 1, 4 * e + 2, 4 * e + 3, 24 + 2 * e, 24 + 2 * e + 1};
      for (int k = 0; k < 6; k++) {
        if (k < 4 ? !n2 : !n3) continue;
        s.kind[s.n] = 1; s.p0[s.n] = bsel[k]; s.p1[s.n] = s.nd; s.ld[s.n] = lsel[k]; s.tile[s.n] = i;
        r.dmap[dm[k]] = s.nd; s.dsrc[s.nd++] = s.n; s.n++;
      }
    }
    for (int m = 0; m < 17; m++) {
      const int pp = 8 * q - m;
      if (pp < -7) continue;
      s.loff[s.nl] = 64 * (pp + 7);
      s.kind[s.n] = 0; s.p0[s.n] = m; s.ld[s.n] = s.nl++; s.tile[s.n] = i; s.n++;
    }
    for (int a = 2; a <= 4; a++)
      for (int h = 0; h < 2; h++)
        if (q - a >= 0) { s.kind[s.n] = 2; s.p0[s.n] = 2 * (q - a) + h; s.p1[s.n] = 2 * (a - 2) + h; s.ld[s.n] = -1; s.tile[s.n] = i; s.n++; }
    for (int a = 4; a <= 7; a++)
      if (q - a >= 0) { s.kind[s.n] = 2; s.p0[s.n] = 12 + (q - a); s.p1[s.n] = 6 + (a - 4); s.ld[s.n] = -1; s.tile[s.n] = i; s.n++; }
    s.last[i] = s.n - 1;
    s.yat[i] = s.n + 1;
  }
  int nl = PFD, nc = 0, mx = 0;
  s.lcum[0] = nl; s.ccum[0] = 0;
  for (int i = 0; i < s.n; i++) {
    if (s.ld[i] > mx) mx = s.ld[i];
    int slots = 2;
    if (nl < s.nl && nl <= (mx | 1) + PFD - 1) { nl++; slots--; }
    while (slots > 0 && nc < 2 * s.nd && s.dsrc[nc / 2] <= i - 2) { nc++; slots--; }
    if (slots > 0 && nl < s.nl && nl <= (mx | 1) + PFD - 1) { nl++; slots--; }
    s.lcum[i + 1] = nl; s.ccum[i + 1] = nc;
  }
  return r;
}
template <int HF> struct SchedOf { static constexpr Sched2 v = make_sched(HF); };

struct ConvRegs { bf8 An[17], T[10], bands[5]; };
template <int HF, int I>
DEVI void conv_step(u32 ub, u32 yaddr, u32 faddr, const ConvRegs& R, bf8 (&fr)[96], f4 (&D)[40], u32 (&P)[80], f4 (&acc)[4]) {
  constexpr const Sched2& S2 = SchedOf<HF>::v; constexpr const Sched& S = S2.s;
  if constexpr (I < S.n) {
    const f4 z = {0.f, 0.f, 0.f, 0.f};
    constexpr int t = S.tile[I], p0 = S.p0[I], p1 = S.p1[I], ld = S.ld[I];
    if constexpr (S.kind[I] == 0) acc[t] = mma(R.An[p0], fr[ld], p0 == 0 ? z : acc[t]);
    else if constexpr (S.kind[I] == 1) D[p1] = mma(fr[ld], R.bands[p0], z);
    else {
      constexpr int d0 = S2.dmap[2 * S.p0[I]], d1 = S2.dmap[2 * S.p0[I] + 1];
      static_assert(S.ccum[I] >= 2 * d0 + 2 && S.ccum[I] >= 2 * d1 + 2, "window not converted in time");
      acc[t] = mma(__builtin_bit_cast(bf8, ((u4v){P[2 * d0], P[2 * d0 + 1], P[2 * d1], P[2 * d1 + 1]})), R.T[p1], acc[t]);
    }
    __builtin_amdgcn_sched_barrier(0);
    constexpr int l0 = S.lcum[I], l1 = S.lcum[I + 1], c0 = S.ccum[I], c1 = S.ccum[I + 1];
    if constexpr (l0 < l1) { constexpr int k = lswap(l0, S.nl), o = S.loff[k]; fr[k] = lds_ld<bf8>(ub + o); }
    if constexpr (l0 + 1 < l1) { constexpr int k = lswap(l0 + 1, S.nl), o = S.loff[k]; fr[k] = lds_ld<bf8>(ub + o); }
    if constexpr (c0 < c1) P[c0] = pk2(D[c0 / 2][2 * (c0 & 1)], D[c0 / 2][2 * (c0 & 1) + 1]);
    if constexpr (c0 + 1 < c1) P[c0 + 1] = pk2(D[(c0 + 1) / 2][2 * ((c0 + 1) & 1)], D[(c0 + 1) / 2][2 * ((c0 + 1) & 1) + 1]);
    constexpr int yi = S.yat[0] == I ? 0 : S.yat[1] == I ? 1 : S.yat[2] == I ? 2 : I == S.n - 1 ? 3 : -1;
    if constexpr (yi >= 0) {
      const f4 y = acc[yi] + 0.0f;
      asm volatile("ds_write_b128 %0, %1 offset:%2\n\tds_write_b32 %3, %4 offset:%5" :: "v"(yaddr), "v"(y), "i"(yi * 1024), "v"(faddr), "v"(yaddr), "i"(yi * 16));
    }
    __builtin_amdgcn_sched_barrier(0);
    conv_step<HF, I + 1>(ub, yaddr, faddr, R, fr, D, P, acc);
  }
}
template <int HF, int L>
DEVI void conv_pre(u32 ub, bf8 (&fr)[96]) {
  if constexpr (L < PFD) { constexpr int k = lswap(L, 99), o = SchedOf<HF>::v.s.loff[k]; fr[k] = lds_ld<bf8>(ub + o); conv_pre<HF, L + 1>(ub, fr); }
}
template <int HF>
DEVI void conv(u32 ub, u32 yaddr, u32 faddr, const ConvRegs& R) {
  bf8 fr[96]; f4 D[40]; u32 P[80]; f4 acc[4];
  conv_pre<HF, 0>(ub, fr);
  __builtin_amdgcn_sched_barrier(0);
  conv_step<HF, 0>(ub, yaddr, faddr, R, fr, D, P, acc);
}

__global__ void __launch_bounds__(512, 2)
s4_moe_kernel(const u16* x_, const u16* Are_, const u16* Aim_, const u16* Bre_, const u16* Bim_, const u16* Cre_,
              const u16* Cim_, const u16* ldt_, const u16* dsk_, const u16* wr_, const u16* wg_, const u16* wu_,
              const u16* wd_, u16* out_, char* ws_, u64 pad_) {
  extern __shared__ __attribute__((aligned(16))) char smem[];
  const int lane = threadIdx.x & 63;
  const int wid = __builtin_amdgcn_readfirstlane(threadIdx.x >> 6);
  const int w = blockIdx.x, xc = w & 7, mm = w >> 3;
  const int cg = xc * 16 + (mm >> 1), hf = mm & 1;
  const bool prod = mm < 14;
  STAMP_DECL;
  STAMP(0);
  typedef u32 u16v __attribute__((ext_vector_type(16)));
  u16v ka, kb_;
  asm volatile("s_load_dwordx16 %0, %2, 0x0\n\ts_load_dwordx16 %1, %2, 0x40\n\ts_waitcnt lgkmcnt(0)" : "=&s"(ka), "=&s"(kb_) : "s"(__builtin_amdgcn_kernarg_segment_ptr()) : "memory");
  STAMP(14);
#define KP(T, v, i) ((T)(((u64)v[2 * (i) + 1] << 32) | v[2 * (i)]))
  const u16* x = KP(const u16*, ka, 0); const u16* Are = KP(const u16*, ka, 1); const u16* Aim = KP(const u16*, ka, 2); const u16* Bre = KP(const u16*, ka, 3);
  const u16* Bim = KP(const u16*, ka, 4); const u16* Cre = KP(const u16*, ka, 5); const u16* Cim = KP(const u16*, ka, 6); const u16* ldt = KP(const u16*, ka, 7);
  const u16* dsk = KP(const u16*, kb_, 0); const u16* wr = KP(const u16*, kb_, 1); const u16* wg = KP(const u16*, kb_, 2); const u16* wu = KP(const u16*, kb_, 3);
  const u16* wd = KP(const u16*, kb_, 4); u16* out = KP(u16*, kb_, 5); char* ws = KP(char*, kb_, 6);

  if (threadIdx.x == 0) __hip_atomic_store((GAS u32*)(ws + W_STAMP) + w, (u32)__builtin_amdgcn_s_getreg(20 | (3 << 11)) & 0xfu, __ATOMIC_RELAXED, __HIP_MEMORY_SCOPE_AGENT);
  if (wid < 4) {
    __builtin_amdgcn_s_setprio(2);
    const int d = 4 * cg + wid;
    u2v xt0, xt1, xt2, xt3, xt4, xt5, xt6, xt7;
    const int R = xc * 4 + (mm >> 3), S = mm & 7;
    if (wid < 2) {
      const u32 xp = (64 * R + 8 * (lane & 7)) * 1024 + 128 * S + 64 * wid + 8 * (lane >> 3), xq = xp + 4096;
      GLD2_NT(xt0, xp, x, 0); GLD2_NT(xt1, xp, x, 1024); GLD2_NT(xt2, xp, x, 2048); GLD2_NT(xt3, xp, x, 3072);
      GLD2_NT(xt4, xq, x, 0); GLD2_NT(xt5, xq, x, 1024); GLD2_NT(xt6, xq, x, 2048); GLD2_NT(xt7, xq, x, 3072);
    }
    const u32 po = d * 256 + lane * 4;
    u32 p_ar, p_ai, p_br, p_bi, p_cr, p_ci, p_dt;
    GLDH(p_dt, 2 * d, ldt);
    GLD1_NT(p_ar, po, Are); GLD1_NT(p_ai, po, Aim); GLD1_NT(p_br, po, Bre); GLD1_NT(p_bi, po, Bim); GLD1_NT(p_cr, po, Cre); GLD1_NT(p_ci, po, Cim);
    if (wid >= 2) {
      u64 pc; asm volatile("s_getpc_b64 %0" : "=s"(pc));
      const u32 ln = (wid - 2) * 64 + lane;
      dma4((ln < CODE_LINES ? ln : 0) * 128, (const void*)pc, M_DUM);
      dma4(0u, out + 4 * cg, M_DUM);
    }
    if (lane < 32) lds_st<u4v>(UB + wid * UCH + lane * 16, (u4v){0, 0, 0, 0});
    asm volatile("s_waitcnt lgkmcnt(0)\n\ts_barrier" ::: "memory");
    STAMP(6);
    ConvRegs CR;
    CR.bands[0] = band(lane, 8, 2); CR.bands[1] = band(lane, 12, 1); CR.bands[2] = band(lane, 4, 1); CR.bands[3] = band(lane, 8, 4); CR.bands[4] = band(lane, 8, 3);
    const u32 kb = wid * KREG;
    const int hh = lane >> 5, j = lane & 31, od = lane & 1, wh = lane >> 2;
    u32 a0 = kb + 32 + 16 * j + 8 * hh;
    u32 a1 = kb + (lane < 36 ? 544 + 16 * lane : lane < 38 ? 16 * (lane - 36) : O_Y);
    u32 tA = kb + (hh ? (od ? O_TB + 32 : O_TA + 30) : (od ? O_TF + 32 : O_TE + 30)) + (j >> 1) * 2;
    u32 tB = hh ? kb + (od ? O_TD : O_TC) + 32 + (j >> 1) * 2 : tA + 64;
    constexpr u64 SAT = (u64)(O_TA + 30) | ((u64)(O_TE + 30) << 12) | ((u64)(O_TA + 94) << 24) | ((u64)(O_TC + 96) << 36) | ((u64)(O_TE + 158) << 48);
    u32 sa = kb + (lane < 20 ? (lane & 3) * (CS + 2) + (u32)((SAT >> (12 * (wh < 5 ? wh : 0))) & 0xfff) : O_Y);
    u32 zA = kb + (lane < 52 ? O_TA + lane * 16 : 1952);
    u32 cA = kb + 16 * lane, pA = lane ? cA - 8 : cA, cA2 = kb + (lane < 6 ? 1024 + 16 * lane : 1952);
    u32 m1 = lane == 32 ? 0xffffffffu : 0u, m0 = hh ? 0u : 0xffffffffu;
    u32 bn = kb + CS * (wh & 3) + 56 - 16 * (lane >> 4) + 8 * (lane & 3), bt = kb + CS * (wh & 3) - 8 * (lane >> 4) + 8 * (lane & 3);
    u32 ta = kb + lane * 4, fa = kb + j * TROW + hh * 16;
    asm volatile("" : "+v"(CR.bands[0]), "+v"(CR.bands[1]), "+v"(CR.bands[2]), "+v"(CR.bands[3]), "+v"(CR.bands[4]), "+v"(a0), "+v"(a1), "+v"(tA), "+v"(tB),
                 "+v"(sa), "+v"(zA), "+v"(cA), "+v"(pA), "+v"(cA2), "+v"(m1), "+v"(m0), "+v"(bn), "+v"(bt), "+v"(ta), "+v"(fa));
    STAMP(7);
    if (wid >= 2) {
      __builtin_amdgcn_s_sleep(SLP_W);
      const u32 row = mm * 16 + (lane >> 2);
      const u32 vo = row * 3584 + ((xc * 448) & ~127) + (lane & 3) * 128;
      dma4nt(vo, wid == 2 ? wg : wu, M_DUM);
    } else {
      u2v tg; u32 spins = 0;
      do { tg = lds_ldv<u2v>(M_TAG); } while (tg.y == 0 && ++spins < SPIN_CAP);
      STAMP(8);
      asm volatile("s_waitcnt vmcnt(7)" : "+v"(xt0), "+v"(xt1), "+v"(xt2), "+v"(xt3), "+v"(xt4), "+v"(xt5), "+v"(xt6), "+v"(xt7) :: "memory");
      STAMP(9);
      const u2v xt[8] = {xt0, xt1, xt2, xt3, xt4, xt5, xt6, xt7};
      u32 c = tg.x;
#pragma unroll
      for (int i = 0; i < 8; i++) c += xt[i].x + xt[i].y;
      const __amdgpu_buffer_rsrc_t rs = __builtin_amdgcn_make_buffer_rsrc(ws + W_XB + (R * 8 + S) * XBS, 0, XBS, 0x00020000);
      const u32 o = (8 * wid + (lane >> 3)) * 512 + (lane & 7) * 16;
#pragma unroll
      for (int i = 0; i < 4; i++)
        __builtin_amdgcn_raw_buffer_store_b128((u4v){xt[2 * i].x, xt[2 * i].y, xt[2 * i + 1].x, xt[2 * i + 1].y}, rs, o + i * 128, 0, 16);
      __builtin_amdgcn_raw_buffer_store_b32(c, rs, 8192 + wid * 256 + lane * 4, 0, 16);
    }
    asm volatile("s_cmp_lt_u32 %7, 2\n\ts_cbranch_scc1 1f\n\ts_waitcnt vmcnt(3)\n1:\n\ts_waitcnt vmcnt(5)"
                 : "+v"(p_ar), "+v"(p_ai), "+v"(p_br), "+v"(p_bi), "+v"(p_cr), "+v"(p_ci), "+v"(p_dt) : "s"(wid) : "memory", "scc");
    STAMP(1);

    const float dt = rbf(__builtin_amdgcn_exp2f(bl(p_dt) * 1.44269504f));
    f16v kd = {};
#pragma unroll 1
    for (int h = 0; h < 2; h++) {
      const u32 sh = 16 - 16 * h;
      const float a2 = dt * bh(p_ar << sh) * 1.44269504f, rv = dt * bh(p_ai << sh) * 0.159154943f;
      const f2 Bc = {dt * bh(p_br << sh), dt * bh(p_bi << sh)}, Cc = {bh(p_cr << sh), bh(p_ci << sh)};
      const f2 G = cmul(Cc, Bc, rot(Bc));
      const f2 w1 = cexp(a2, rv, 1.f), w256 = cexp(a2, rv, 256.f);
      f2 v8 = cexp(a2, -rv, 8.f);
      const f2 w1r = rot(w1), w256r = rot(w256);
      const f2 w2 = cmul(w1, w1, w1r), w2r = rot(w2), w4 = cmul(w2, w2, w2r), w4r = rot(w4);
      const f2 v16 = cmul(v8, v8, rot(v8)), v16r = rot(v16);
#define TW(r, v) lds_st<u32>(ta + (r) * TROW, pk2((v).x, (v).y))
      f2 fz = G, ge = v16, go = v8, fh = cmul(G, w256, w256r);
      TW(0, fz); TW(21, ((f2){1.f, 0.f})); TW(22, go); TW(23, ge); TW(8, fh);
      f2 t2 = cmul(fh, w2, w2r); TW(9, t2); t2 = cmul(t2, w2, w2r); TW(10, t2); t2 = cmul(t2, w2, w2r); TW(11, t2);
#pragma unroll
      for (int i = 1; i < 16; i++) {
        go = cmul(go, v16, v16r); TW(22 + 2 * i, go);
        if (i < 15) { ge = cmul(ge, v16, v16r); TW(23 + 2 * i, ge); }
        if (i < 8) { fz = cmul(fz, w1, w1r); TW(i, fz); }
        else {
          fh = cmul(fh, w256, w256r);
          if (i < 10) { TW(12 + (i - 8), fh); f2 t4 = cmul(fh, w4, w4r); TW(14 + (i - 8), t4); }
          else if (i < 15) TW(16 + (i - 10), fh);
        }
      }
      bf8 tf[16];
#pragma unroll
      for (int s = 0; s < 8; s++) { tf[2 * s] = lds_ld<bf8>(fa + 32 * s); tf[2 * s + 1] = lds_ld<bf8>(fa + 21 * TROW + 32 * s); }
      __builtin_amdgcn_sched_barrier(0);
#pragma unroll
      for (int s = 0; s < 8; s++) kd = __builtin_amdgcn_mfma_f32_32x32x16_bf16(tf[2 * s], tf[2 * s + 1], kd, 0, 0, 0);
    }
#pragma unroll
    for (int q = 0; q < 4; q++) lds_st<u4v>(zA + q * CS, (u4v){0, 0, 0, 0});
    STAMP(2);
    {
      lds_st<u2v>(a0, (u2v){pk2(kd[0], kd[1]), pk2(kd[2], kd[3])});
      const float en = DPPF(kd[4], 0x130, 0xf);
      const u32 e00 = pk2(kd[4], kd[4]);
      lds_st<u4v>(a1, (u4v){e00 & (m0 | m1), (pk2(kd[5], kd[6]) & m0) | (e00 & m1 & 0xffffu), pk2(kd[6], kd[6]) & m0, pk2(kd[7], en) & m0});
      const u4v c = lds_ld<u4v>(cA), c2 = lds_ld<u4v>(cA2);
      const u2v p = lds_ld<u2v>(pA), p2 = lds_ld<u2v>(cA2 - 8);
      const u32 v01 = hh ? pk2(kd[4], kd[5]) : pk2(kd[8], kd[9]), v23 = hh ? pk2(kd[6], kd[7]) : pk2(kd[10], kd[11]);
#pragma unroll
      for (int q = 0; q < 4; q++) {
        lds_st<u16>(tA + q * (CS + 2), (u16)v01); lds_st<u16>(tA + q * (CS + 2) + 32, (u16)(v01 >> 16));
        lds_st<u16>(tB + q * (CS + 2), (u16)v23); lds_st<u16>(tB + q * (CS + 2) + 32, (u16)(v23 >> 16));
      }
      const u32 k8 = pk2(kd[8], kd[8]);
      const u32 al = rl(k8, 0), k2048 = rl(k8, 32);
      lds_st<u16>(sa, (u16)(lane < 8 ? 0u : lane < 16 ? al : k2048));
      lds_st<u32>(M_AL + wid * 4, al << 16);
#define SHIFTS(c, p, A) do { \
      const u32 s0 = __builtin_amdgcn_alignbit(p.y, p.x, 16), s1 = __builtin_amdgcn_alignbit(c.x, p.y, 16), s2 = __builtin_amdgcn_alignbit(c.y, c.x, 16), \
                s3 = __builtin_amdgcn_alignbit(c.z, c.y, 16), s4 = __builtin_amdgcn_alignbit(c.w, c.z, 16); \
      lds_st<u4v>(A + CS, (u4v){s1, s2, s3, s4}); lds_st<u4v>(A + 2 * CS, (u4v){p.y, c.x, c.y, c.z}); lds_st<u4v>(A + 3 * CS, (u4v){s0, s1, s2, s3}); } while (0)
      SHIFTS(c, p, cA);
      SHIFTS(c2, p2, cA2);
    }
    MEMF();
#pragma unroll
    for (int a = 2; a <= 4; a++) {
      CR.T[2 * (a - 2)] = cat(trd(bt + O_TA + 32 * a - 32), trd(bt + O_TB + 32 * a - 32));
      CR.T[2 * (a - 2) + 1] = cat(trd(bt + O_TC + 32 * a - 32), trd(bt + O_TD + 32 * a - 32));
    }
#pragma unroll
    for (int a = 4; a <= 7; a++) CR.T[6 + a - 4] = cat(trd(bt + O_TE + 32 * a - 96), trd(bt + O_TF + 32 * a - 96));
#pragma unroll
    for (int m = 0; m < 17; m++) CR.An[m] = cat(trd(bn + 64 * m + 8), trd(bn + 64 * m));
    u32 ub = UB + wid * UCH + 32 + 32 * (lane & 15) + 16 * (lane >> 4);
    u32 ya = kb + O_Y + 64 * (lane & 15) + 16 * (lane >> 4), fl = M_YF + wid * 4;
    asm volatile("" : "+v"(ub), "+v"(ya), "+v"(fl));
    STAMP(3);
    asm volatile("s_waitcnt lgkmcnt(0)\n\ts_barrier" ::: "memory");
    STAMP(4);
    if (hf == 0) conv<0>(ub, ya, fl, CR); else conv<1>(ub, ya, fl, CR);
    STAMP(5);
    asm volatile("s_waitcnt vmcnt(0) lgkmcnt(0)" ::: "memory");
    STAMP_FLUSH();
    return;
  }

  const int cw = wid - 4;
  const int sp = xc * 32 + mm;
  u32 ep = 0;
  if (cw == 0) ep = __hip_atomic_load((GAS u32*)(ws + W_EP), __ATOMIC_RELAXED, __HIP_MEMORY_SCOPE_AGENT);
  const u4v xr0 = __builtin_nontemporal_load((const GAS u4v*)(x + (size_t)(8 * sp + 2 * cw) * 512) + lane);
  const u4v xr1 = __builtin_nontemporal_load((const GAS u4v*)(x + (size_t)(8 * sp + 2 * cw + 1) * 512) + lane);
  const u4v wv = ((const GAS u4v*)wr)[lane];
  const u2v dk = *(const GAS u2v*)(dsk + 4 * cg);
  if (cw == 3) lds_st<u32>(MB + lane * 4, 0u);
  asm volatile("s_waitcnt lgkmcnt(0)\n\ts_barrier" ::: "memory");
  u32 tag;
  if (cw == 0) {
    tag = __builtin_amdgcn_readfirstlane(ep) + 1u;
    if (lane == 0) lds_stv<u2v>(M_TAG, (u2v){tag, 1u});
  }
  {
    const bf2 ones = __builtin_bit_cast(bf2, 0x3F803F80u);
    float s0 = 0, q0 = 0, d0 = 0, s1 = 0, q1 = 0, d1 = 0, sw = 0;
#pragma unroll
    for (int i = 0; i < 4; i++) {
      const bf2 a = asbf2(xr0[i]), b = asbf2(xr1[i]), c = asbf2(wv[i]);
      s0 = __builtin_amdgcn_fdot2_f32_bf16(a, ones, s0, false); q0 = __builtin_amdgcn_fdot2_f32_bf16(a, a, q0, false); d0 = __builtin_amdgcn_fdot2_f32_bf16(a, c, d0, false);
      s1 = __builtin_amdgcn_fdot2_f32_bf16(b, ones, s1, false); q1 = __builtin_amdgcn_fdot2_f32_bf16(b, b, q1, false); d1 = __builtin_amdgcn_fdot2_f32_bf16(b, c, d1, false);
      sw = __builtin_amdgcn_fdot2_f32_bf16(c, ones, sw, false);
    }
    auto sw32 = [](float a, float b) { auto r = __builtin_amdgcn_permlane32_swap(__float_as_uint(a), __float_as_uint(b), false, false); return __uint_as_float(r[0]) + __uint_as_float(r[1]); };
    float s = sw32(s0, s1), q = sw32(q0, q1), dd = sw32(d0, d1); sw = sw32(sw, sw);
#define RED(v) v += DPPF(v, 0x128, 0xf);
    s += DPPF(s, 0x128, 0xf); q += DPPF(q, 0x128, 0xf); dd += DPPF(dd, 0x128, 0xf); sw += DPPF(sw, 0x128, 0xf);
    s += DPPF(s, 0x124, 0xf); q += DPPF(q, 0x124, 0xf); dd += DPPF(dd, 0x124, 0xf); sw += DPPF(sw, 0x124, 0xf);
    s += DPPF(s, 0x122, 0xf); q += DPPF(q, 0x122, 0xf); dd += DPPF(dd, 0x122, 0xf); sw += DPPF(sw, 0x122, 0xf);
    s += DPPF(s, 0x121, 0xf); q += DPPF(q, 0x121, 0xf); dd += DPPF(dd, 0x121, 0xf); sw += DPPF(sw, 0x121, 0xf);
    s += DPPF(s, 0x142, 0xa); q += DPPF(q, 0x142, 0xa); dd += DPPF(dd, 0x142, 0xa); sw += DPPF(sw, 0x142, 0xa);
    const float mean = s * (1.f / 512.f);
    const float var = fmaxf(q * (1.f / 512.f) - mean * mean, 0.f);
    const float mb = rbf(mean), sb = rbf(__builtin_amdgcn_sqrtf(rbf(rbf(var) + 1e-5f)));
    const float r = (dd - mb * sw) * __builtin_amdgcn_rcpf(sb);
    const u32 ms = pk2(mb, sb), rb = pk2(r, r) & 0xffffu;
    const u32 ms0 = rl(ms, 31), ms1 = rl(ms, 63), rr = rl(rb, 31) | (rl(rb, 63) << 16);
    if (cw != 0) {
      u2v tg; u32 spins = 0;
      do { tg = lds_ldv<u2v>(M_TAG); } while (tg.y == 0 && ++spins < SPIN_CAP);
      tag = __builtin_amdgcn_readfirstlane(tg.x);
    }
    const __amdgpu_buffer_rsrc_t rs = __builtin_amdgcn_make_buffer_rsrc(ws + W_ST, 0, 16384, 0x00020000);
    if (lane == 0) __builtin_amdgcn_raw_buffer_store_b128((u4v){ms0, ms1, rr, ms0 + ms1 + rr + tag + tag - 1u}, rs, cw * 4096 + sp * 16, 0, 16);
  }
  STAMP(1);
  const u32 chs = tag + tag - 1u;
  u4v su0, su1, su2, su3, xu0, xu1, xu2, xu3; u32 xk;
  const u32 so = W_ST + (64 * cw + lane) * 16;
  const u32 xb = W_XB + ((8 * cw + (lane >> 3)) * 8 + xc) * XBS;
  const u32 xo = xb + (mm >> 1) * 512 + (lane & 7) * 16, xko = xb + 8192 + (mm >> 1) * 32 + (lane & 7) * 4;
  const char* ws1 = ws + 4096; const char* ws2 = ws + 8192; const char* ws3 = ws + 12288;
#define LOAD_S() do { GLD4_SC1(su0, so, ws, 0); GLD4_SC1(su1, so, ws1, 0); GLD4_SC1(su2, so, ws2, 0); GLD4_SC1(su3, so, ws3, 0); } while (0)
#define LOAD_X() do { GLD4_SC1(xu0, xo, ws, 0); GLD4_SC1(xu1, xo, ws, 128); GLD4_SC1(xu2, xo, ws, 256); GLD4_SC1(xu3, xo, ws, 384); GLD1_SC1(xk, xko, ws, 0); } while (0)
#define OK_S() ((((su0.x + su0.y + su0.z + chs) ^ su0.w) | ((su1.x + su1.y + su1.z + chs) ^ su1.w) | ((su2.x + su2.y + su2.z + chs) ^ su2.w) | ((su3.x + su3.y + su3.z + chs) ^ su3.w)) == 0)
#define SUM4(v) (v.x + v.y + v.z + v.w)
#define OK_X() (SUM4(xu0) + SUM4(xu1) + SUM4(xu2) + SUM4(xu3) + tag == xk)
  __builtin_amdgcn_s_sleep(SLP_S);
  LOAD_S();
  __builtin_amdgcn_s_sleep(SLP_X);
  LOAD_X();
  const u32 fb = FB + cw * 12288;
  const int fp = xc * 14 + mm;
#define PULL() do { \
    const u32 go = (128 * cw + (lane >> 1)) * 3584 + fp * 32 + (lane & 1) * 16; \
    constexpr u32 GS = 32 * 3584 - 1024; \
    dma16x4(go, go + GS, go + 2 * GS, go + 3 * GS, wg, fb); \
    dma16x4(go, go + GS, go + 2 * GS, go + 3 * GS, wu, fb + 4096); \
    const u32 dof = (16 * fp + (lane >> 4)) * 1024 + 256 * cw + (lane & 15) * 16; \
    dma16x4(dof, dof + 3072, dof + 6144, dof + 9216, wd, fb + 8192); } while (0)
  asm volatile("s_waitcnt vmcnt(5)" : "+v"(su0), "+v"(su1), "+v"(su2), "+v"(su3) :: "memory");
  STAMP(2);
  for (u32 spins = 0; !__all(OK_S()) && spins < SPIN_CAP; spins++) {
    asm volatile("s_sleep 2\n\tglobal_load_dwordx4 %0, %4, %5 sc1\n\tglobal_load_dwordx4 %1, %4, %6 sc1\n\tglobal_load_dwordx4 %2, %4, %7 sc1\n\t"
                 "global_load_dwordx4 %3, %4, %8 sc1\n\ts_waitcnt vmcnt(0)"
                 : "=&v"(su0), "=&v"(su1), "=&v"(su2), "=&v"(su3) : "v"(so), "s"(ws), "s"(ws1), "s"(ws2), "s"(ws3) : "memory");
  }
  STAMP(3);
  u32 k1, k2;
  {
    const u32 ib = 0xffffu - (512 * cw + 8 * lane);
    auto key = [&](u32 b, int i) { u32 o = b ^ ((u32)((int)b >> 31) | 0x80000000u); return (o & 0xffff0000u) | (ib - i); };
    k1 = key(su0.z << 16, 0); k2 = 0;
    auto ins = [&](u32 k) { k2 = umax(k2, umin(k1, k)); k1 = umax(k1, k); };
    ins(key(su0.z & 0xffff0000u, 1)); ins(key(su1.z << 16, 2)); ins(key(su1.z & 0xffff0000u, 3)); ins(key(su2.z << 16, 4));
    ins(key(su2.z & 0xffff0000u, 5)); ins(key(su3.z << 16, 6)); ins(key(su3.z & 0xffff0000u, 7));
    const u32 K1 = wave_umax(k1), K2 = wave_umax(k1 == K1 ? k2 : k1);
    if (lane == 0) lds_stv<u2v>(M_TOP + cw * 8, (u2v){K1, K2});
    u4v ta, tb; u32 spins = 0;
    do { ta = lds_ldv<u4v>(M_TOP); tb = lds_ldv<u4v>(M_TOP + 16); } while ((ta.x == 0 || ta.z == 0 || tb.x == 0 || tb.z == 0) && ++spins < SPIN_CAP);
    const u32 a1 = umax(ta.x, ta.z), a2 = umax(umin(ta.x, ta.z), umax(ta.y, ta.w)), b1 = umax(tb.x, tb.z), b2 = umax(umin(tb.x, tb.z), umax(tb.y, tb.w));
    k1 = __builtin_amdgcn_readfirstlane(umax(a1, b1)); k2 = __builtin_amdgcn_readfirstlane(umax(umin(a1, b1), umax(a2, b2)));
  }
  const int t0 = (0xffffu - (k1 & 0xffffu)) & 2047, t1 = (0xffffu - (k2 & 0xffffu)) & 2047;
  u4v xs0, xs1, xs2, xs3;
  if (prod) {
    const u32 xso = ((lane & 1) ? t1 : t0) * 1024 + 256 * cw + 16 * (lane >> 4);
    GLD4(xs0, xso, x, 0); GLD4(xs1, xso, x, 64); GLD4(xs2, xso, x, 128); GLD4(xs3, xso, x, 192);
  }
  if (w == 0 && cw == 0 && lane == 0) __hip_atomic_store((GAS u32*)(ws + W_EP), tag, __ATOMIC_RELAXED, __HIP_MEMORY_SCOPE_AGENT);
  if ((t0 >> 3) == 64 * cw + lane) { lds_st<u4v>(M_SEL, (u4v){su0.x, su0.y, su1.x, su1.y}); lds_st<u4v>(M_SEL + 16, (u4v){su2.x, su2.y, su3.x, su3.y}); }
  if ((t1 >> 3) == 64 * cw + lane) { lds_st<u4v>(M_SEL + 32, (u4v){su0.x, su0.y, su1.x, su1.y}); lds_st<u4v>(M_SEL + 48, (u4v){su2.x, su2.y, su3.x, su3.y}); }
  float na[8], nb[8];
  {
    const u32 msv[8] = {su0.x, su0.y, su1.x, su1.y, su2.x, su2.y, su3.x, su3.y};
#pragma unroll
    for (int i = 0; i < 8; i++) { na[i] = __builtin_amdgcn_rcpf(bh(msv[i])); nb[i] = -bl(msv[i]) * na[i]; }
  }
  asm volatile("" : "+v"(na[0]), "+v"(na[1]), "+v"(na[2]), "+v"(na[3]), "+v"(na[4]), "+v"(na[5]), "+v"(na[6]), "+v"(na[7]),
               "+v"(nb[0]), "+v"(nb[1]), "+v"(nb[2]), "+v"(nb[3]), "+v"(nb[4]), "+v"(nb[5]), "+v"(nb[6]), "+v"(nb[7]));
  STAMP(4);
#define XW "4"
  asm volatile("s_cmp_lt_u32 %5, 111\n\ts_cbranch_scc1 1f\n\ts_waitcnt vmcnt(0)\n1:\n\ts_waitcnt vmcnt(" XW ")"
               : "+v"(xu0), "+v"(xu1), "+v"(xu2), "+v"(xu3), "+v"(xk) : "s"(w - 1) : "memory", "scc");
  for (u32 spins = 0; !__all(OK_X()) && spins < SPIN_CAP; spins++) {
    asm volatile("s_sleep 1\n\tglobal_load_dwordx4 %0, %5, %7 sc1\n\tglobal_load_dwordx4 %1, %5, %7 offset:128 sc1\n\tglobal_load_dwordx4 %2, %5, %7 offset:256 sc1\n\t"
                 "global_load_dwordx4 %3, %5, %7 offset:384 sc1\n\tglobal_load_dword %4, %6, %7 sc1\n\ts_waitcnt vmcnt(0)"
                 : "=&v"(xu0), "=&v"(xu1), "=&v"(xu2), "=&v"(xu3), "=&v"(xk) : "v"(xo), "v"(xko), "s"(ws) : "memory");
  }
  STAMP(5);
  u32 xa[8] = {xu0.x, xu0.z, xu1.x, xu1.z, xu2.x, xu2.z, xu3.x, xu3.z};
  u32 xcv[8] = {xu0.y, xu0.w, xu1.y, xu1.w, xu2.y, xu2.w, xu3.y, xu3.w};
  {
    u4v c0, c1, c2, c3;
#pragma unroll
    for (int i = 0; i < 4; i++) {
      const int r0 = 2 * i, r1 = 2 * i + 1;
      const f2 p0 = __builtin_elementwise_fma((f2){bl(xa[r0]), bh(xa[r0])}, (f2){na[r0], na[r0]}, (f2){nb[r0], nb[r0]});
      const f2 p1 = __builtin_elementwise_fma((f2){bl(xa[r1]), bh(xa[r1])}, (f2){na[r1], na[r1]}, (f2){nb[r1], nb[r1]});
      const f2 p2 = __builtin_elementwise_fma((f2){bl(xcv[r0]), bh(xcv[r0])}, (f2){na[r0], na[r0]}, (f2){nb[r0], nb[r0]});
      const f2 p3 = __builtin_elementwise_fma((f2){bl(xcv[r1]), bh(xcv[r1])}, (f2){na[r1], na[r1]}, (f2){nb[r1], nb[r1]});
      c0[i] = pk2(p0.x, p1.x); c1[i] = pk2(p0.y, p1.y); c2[i] = pk2(p2.x, p3.x); c3[i] = pk2(p2.y, p3.y);
    }
    const u32 ua = UB + 512 + (64 * cw + lane) * 16;
    lds_st<u4v>(ua, c0); lds_st<u4v>(ua + UCH, c1); lds_st<u4v>(ua + 2 * UCH, c2); lds_st<u4v>(ua + 3 * UCH, c3);
  }
  asm volatile("s_waitcnt lgkmcnt(0)\n\ts_barrier" ::: "memory");
  STAMP(6);

  if (prod) {
    PULL();
    const u32 msel = lds_ld<u32>(M_SEL + (lane & 1) * 32 + (((lane & 1) ? t1 : t0) & 7) * 4);
    const float a = __builtin_amdgcn_rcpf(bh(msel)), b = -bl(msel) * a;
    asm volatile("s_waitcnt vmcnt(0)" : "+v"(xs0), "+v"(xs1), "+v"(xs2), "+v"(xs3) :: "memory");
    STAMP(7);
    auto nrm = [&](u4v v) { u4v r;
#pragma unroll
      for (int i = 0; i < 4; i++) r[i] = pk2(fmaf(bl(v[i]), a, b), fmaf(bh(v[i]), a, b));
      return __builtin_bit_cast(bf8, r); };
    const bf8 xf[4] = {nrm(xs0), nrm(xs1), nrm(xs2), nrm(xs3)};
    const int g = lane >> 4, q = (lane >> 2) & 3, p = lane & 3;
    const u32 ga = fb + (8 * g + q) * 32 + 8 * p;
    f4 ag = {0, 0, 0, 0}, au = {0, 0, 0, 0};
#pragma unroll
    for (int s = 0; s < 4; s++) {
      ag = mma(cat(trd(ga + s * 1024), trd(ga + s * 1024 + 128)), xf[s], ag);
      au = mma(cat(trd(ga + 4096 + s * 1024), trd(ga + 4096 + s * 1024 + 128)), xf[s], au);
    }
    if ((lane & 15) < 2) {
      lds_st<f4>(M_PART + cw * 256 + (lane & 1) * 64 + g * 16, ag);
      lds_st<f4>(M_PART + cw * 256 + 128 + (lane & 1) * 64 + g * 16, au);
    }
    MEMF();
    if (lane == 0) lds_stv<u32>(M_FF + cw * 4, 1u);
    u4v ff; u32 spins = 0;
    do { ff = lds_ldv<u4v>(M_FF); } while ((ff.x & ff.y & ff.z & ff.w) == 0 && ++spins < SPIN_CAP);
    MEMF();
    f4 sg = {0, 0, 0, 0}, su = {0, 0, 0, 0};
#pragma unroll
    for (int v = 0; v < 4; v++) {
      sg += lds_ld<f4>(M_PART + v * 256 + (lane & 1) * 64 + g * 16);
      su += lds_ld<f4>(M_PART + v * 256 + 128 + (lane & 1) * 64 + g * 16);
    }
    float hv[4];
#pragma unroll
    for (int i = 0; i < 4; i++) { const float gg = rbf(sg[i]); hv[i] = rbf(gg * sigm(gg)) * rbf(su[i]); }
    const s4v hb = __builtin_bit_cast(s4v, ((u2v){pk2(hv[0], hv[1]), pk2(hv[2], hv[3])}));
    const u32 da = fb + 8192 + g * 1024 + q * 256 + 8 * p;
    const __amdgpu_buffer_rsrc_t rs = __builtin_amdgcn_make_buffer_rsrc(ws + W_FB + fp * 8192, 0, 8192, 0x00020000);
    const u32 fo = (32 * cw + g) * 64 + (lane & 1) * 32 + (lane & 2) * 8;
#pragma unroll
    for (int t = 0; t < 8; t++) {
      const f4 y = __builtin_amdgcn_mfma_f32_16x16x16bf16_1k(__builtin_bit_cast(s4v, trd(da + 32 * t)), hb, (f4){0, 0, 0, 0}, 0, 0, 0);
      const u4v o = {__float_as_uint((lane & 2) ? y[2] : y[0]), tag, __float_as_uint((lane & 2) ? y[3] : y[1]), tag};
      if ((lane & 12) == 0) __builtin_amdgcn_raw_buffer_store_b128(o, rs, fo + t * 256, 0, 16);
    }
    STAMP(8);
  }

  const int up = (cw & 1) ^ hf;
  const int tile = 2 * cw + up;
  const int tl = 8 * (lane & 31) + 4 * (lane >> 5), tr0 = 256 * tile + tl;
  float xv[4][4], uv[4][4], pre[4][4], gt[4][4];
#pragma unroll
  for (int r = 0; r < 4; r++) {
    auto s0 = __builtin_amdgcn_permlane32_swap(xa[r], xa[r + 4], false, false);
    auto s1 = __builtin_amdgcn_permlane32_swap(xcv[r], xcv[r + 4], false, false);
    const u32 e0 = up ? s0[1] : s0[0], e1 = up ? s1[1] : s1[0];
    xv[0][r] = bl(e0); xv[1][r] = bh(e0); xv[2][r] = bl(e1); xv[3][r] = bh(e1);
  }
  {
    const f4 alv = lds_ld<f4>(M_AL);
    const float dv[4] = {bl(dk.x), bh(dk.x), bl(dk.y), bh(dk.y)};
    const u32 ua = UB + 512 + tr0 * 2;
    const u32 ca = tile >= 4 ? ua - 2064 : UB;
#pragma unroll
    for (int c = 0; c < 4; c++) {
      const u2v a = lds_ld<u2v>(ua + c * UCH), p = lds_ld<u2v>(ca + c * UCH), n = lds_ld<u2v>(ca + c * UCH + 8);
      uv[c][0] = bl(a.x); uv[c][1] = bh(a.x); uv[c][2] = bl(a.y); uv[c][3] = bh(a.y);
      pre[c][0] = uv[c][0] * dv[c] - alv[c] * bh(p.y); pre[c][1] = uv[c][1] * dv[c] - alv[c] * bl(n.x);
      pre[c][2] = uv[c][2] * dv[c] - alv[c] * bh(n.x); pre[c][3] = uv[c][3] * dv[c] - alv[c] * bl(n.y);
#pragma unroll
      for (int r = 0; r < 4; r++) gt[c][r] = uv[c][r] * sigm(uv[c][r]);
    }
  }
  STAMP(9);
  {
    u4v yf; u32 spins = 0;
    do { __builtin_amdgcn_s_sleep(1); yf = lds_ldv<u4v>(M_YF + cw * 16); } while (umin(umin(yf.x, yf.y), umin(yf.z, yf.w)) == 0 && ++spins < SPIN_CAP);
  }
  MEMF();
  STAMP(10);
#pragma unroll
  for (int c = 0; c < 4; c++) {
    const f4 y = lds_ld<f4>(c * KREG + O_Y + cw * 1024 + tl * 4);
#pragma unroll
    for (int r = 0; r < 4; r++) pre[c][r] += y[r];
  }
  const bool late = (tr0 >> 2) == (t0 >> 2) || (tr0 >> 2) == (t1 >> 2);
  char* op = (char*)out + (size_t)tr0 * 1024 + cg * 8;
  if (!late) {
#pragma unroll
    for (int r = 0; r < 4; r++)
      *(GAS u2v*)(op + r * 1024) = (u2v){pk2(fmaf(pre[0][r], gt[0][r], xv[0][r]), fmaf(pre[1][r], gt[1][r], xv[1][r])),
                                         pk2(fmaf(pre[2][r], gt[2][r], xv[2][r]), fmaf(pre[3][r], gt[3][r], xv[3][r]))};
  }
  STAMP(11);
  if ((t0 >> 8) == tile || (t1 >> 8) == tile) {
#pragma unroll 1
    for (int k = 0; k < 2; k++) {
      const int tk = k ? t1 : t0; const u32 kk = k ? k2 : k1;
      if ((tk >> 8) != tile) continue;
      const u32 fo = W_FB + (lane >> 1) * 8192 + cg * 64 + k * 32 + (lane & 1) * 16;
      const u32 fo3 = lane < 32 ? fo + 96 * 8192 : fo;
      u4v g0, g1, g2, g3;
      for (u32 spins = 0; spins < SPIN_CAP; spins++) {
        asm volatile("global_load_dwordx4 %0, %4, %8 sc1\n\tglobal_load_dwordx4 %1, %5, %8 sc1\n\tglobal_load_dwordx4 %2, %6, %8 sc1\n\t"
                     "global_load_dwordx4 %3, %7, %8 sc1\n\ts_waitcnt vmcnt(0)"
                     : "=&v"(g0), "=&v"(g1), "=&v"(g2), "=&v"(g3) : "v"(fo), "v"(fo + 32 * 8192), "v"(fo + 64 * 8192), "v"(fo3), "s"(ws) : "memory");
        if (__all(((g0.y ^ tag) | (g0.w ^ tag) | (g1.y ^ tag) | (g1.w ^ tag) | (g2.y ^ tag) | (g2.w ^ tag) | (g3.y ^ tag) | (g3.w ^ tag)) == 0)) break;
        __builtin_amdgcn_s_sleep(1);
      }
      float v0 = __uint_as_float(g0.x) + __uint_as_float(g1.x) + __uint_as_float(g2.x) + (lane < 32 ? __uint_as_float(g3.x) : 0.f);
      float v1 = __uint_as_float(g0.z) + __uint_as_float(g1.z) + __uint_as_float(g2.z) + (lane < 32 ? __uint_as_float(g3.z) : 0.f);
      v0 += DPPF(v0, 0x128, 0xf); v1 += DPPF(v1, 0x128, 0xf); v0 += DPPF(v0, 0x124, 0xf); v1 += DPPF(v1, 0x124, 0xf);
      v0 += DPPF(v0, 0x122, 0xf); v1 += DPPF(v1, 0x122, 0xf);
      float yv[4];
      {
        const u32 a = __float_as_uint(v0), b = __float_as_uint(v1);
        yv[0] = (__uint_as_float(rl(a, 0)) + __uint_as_float(rl(a, 16))) + (__uint_as_float(rl(a, 32)) + __uint_as_float(rl(a, 48)));
        yv[1] = (__uint_as_float(rl(b, 0)) + __uint_as_float(rl(b, 16))) + (__uint_as_float(rl(b, 32)) + __uint_as_float(rl(b, 48)));
        yv[2] = (__uint_as_float(rl(a, 1)) + __uint_as_float(rl(a, 17))) + (__uint_as_float(rl(a, 33)) + __uint_as_float(rl(a, 49)));
        yv[3] = (__uint_as_float(rl(b, 1)) + __uint_as_float(rl(b, 17))) + (__uint_as_float(rl(b, 33)) + __uint_as_float(rl(b, 49)));
      }
      const u32 ob = kk & 0xffff0000u;
      const float rs = rbf(sigm(__uint_as_float((ob & 0x80000000u) ? (ob ^ 0x80000000u) : (~ob & 0xffff0000u))));
#pragma unroll
      for (int c = 0; c < 4; c++) {
        const float upd = rbf(rbf(yv[c]) * rs);
#pragma unroll
        for (int r = 0; r < 4; r++) {
          const float b = rbf(uv[c][r] + upd);
          gt[c][r] = (tr0 + r == tk) ? b * sigm(b) : gt[c][r];
        }
      }
    }
    if (late) {
#pragma unroll
      for (int r = 0; r < 4; r++)
        *(GAS u2v*)(op + r * 1024) = (u2v){pk2(fmaf(pre[0][r], gt[0][r], xv[0][r]), fmaf(pre[1][r], gt[1][r], xv[1][r])),
                                           pk2(fmaf(pre[2][r], gt[2][r], xv[2][r]), fmaf(pre[3][r], gt[3][r], xv[3][r]))};
    }
    STAMP(12);
  }
  asm volatile("s_waitcnt vmcnt(0)" ::: "memory");
  STAMP(13);
  if (blockIdx.x == PW && wid == PWAVE) {
    while (__builtin_amdgcn_s_memrealtime() < pt0 + 2000) {}
    u32 ref = __hip_atomic_load((GAS u32*)(ws + W_STAMP) + (lane & 7), __ATOMIC_RELAXED, __HIP_MEMORY_SCOPE_AGENT);
    int cnt = 0;
    for (int i = 0; i < 4; i++) { u32 v = __hip_atomic_load((GAS u32*)(ws + W_STAMP) + lane + 64 * i, __ATOMIC_RELAXED, __HIP_MEMORY_SCOPE_AGENT); cnt += __popcll(__ballot(v != ref)); }
    ptk = pt0 + cnt;
  }
  STAMP_FLUSH();
}

extern "C" void kernel_launch(void* const* d_in, const int* in_sizes, int n_in, void* d_out, int out_size,
                              void* d_ws, size_t ws_size, hipStream_t stream) {
  (void)hipFuncSetAttribute((const void*)s4_moe_kernel, hipFuncAttributeMaxDynamicSharedMemorySize, LDS_BYTES);
  s4_moe_kernel<<<256, 512, LDS_BYTES, stream>>>(
      (const u16*)d_in[0], (const u16*)d_in[1], (const u16*)d_in[2], (const u16*)d_in[3], (const u16*)d_in[4],
      (const u16*)d_in[5], (const u16*)d_in[6], (const u16*)d_in[7], (const u16*)d_in[8], (const u16*)d_in[10],
      (const u16*)d_in[11], (const u16*)d_in[12], (const u16*)d_in[13], (u16*)d_out, (char*)d_ws, 0ull);
}
```

```cpp
#define KMODE 0
#define PROBE 99
#define PW 200
#define PWAVE 4
#define PULL_LATE
#include <hip/hip_runtime.h>
#include <hip/hip_bf16.h>
#include <stdint.h>

typedef unsigned short u16;
typedef unsigned int u32;
typedef unsigned long long u64;
typedef float f2 __attribute__((ext_vector_type(2)));
typedef float f4 __attribute__((ext_vector_type(4)));
typedef float f16v __attribute__((ext_vector_type(16)));
typedef u32 u2v __attribute__((ext_vector_type(2)));
typedef u32 u4v __attribute__((ext_vector_type(4)));
typedef __bf16 bf2 __attribute__((ext_vector_type(2)));
typedef __bf16 bf4 __attribute__((ext_vector_type(4)));
typedef __bf16 bf8 __attribute__((ext_vector_type(8)));
typedef short s4v __attribute__((ext_vector_type(4)));
#define LAS __attribute__((address_space(3)))
#define GAS __attribute__((address_space(1)))
#define DEVI __device__ __forceinline__

#define SLP_S 10
#define SLP_X 4
#define SLP_W 20
#define SPIN_CAP (1u << 18)

constexpr int KREG = 14592;
constexpr int TROW = 272;
constexpr int CS = 2112;
constexpr int O_TA = 1120, O_TB = 1248, O_TC = 1376, O_TD = 1504, O_TE = 1632, O_TF = 1792;
constexpr int O_Y = 8448;
constexpr int UB = 4 * KREG;
constexpr int UCH = 4608;
constexpr int FB = UB + 4 * UCH;
constexpr int MB = FB + 49152;
constexpr int M_TAG = MB;
constexpr int M_TOP = MB + 16;
constexpr int M_YF = MB + 64;
constexpr int M_FF = MB + 128;
constexpr int M_AL = MB + 144;
constexpr int M_SEL = MB + 192;
constexpr int M_PART = MB + 256;
constexpr int M_DUM = MB + 1280;
constexpr int LDS_BYTES = MB + 1536;

constexpr u32 W_EP = 0;
constexpr u32 W_ST = 4096;
constexpr u32 W_XB = 65536;
constexpr u32 XBS = 8704;
constexpr u32 W_FB = 4u << 20;
constexpr u32 W_STAMP = 8u << 20;

#undef STAMPS
#define STAMP(i) do { if ((i) == 0) asm volatile("s_memrealtime %0\n\ts_waitcnt lgkmcnt(0)" : "=s"(pt0) :: "memory"); if ((i) == PROBE) asm volatile("s_memrealtime %0\n\ts_waitcnt lgkmcnt(0)" : "=s"(ptk) :: "memory"); } while (0)
#define STAMP_DECL u64 pt0 = 0, ptk = 0
#define STAMP_FLUSH() do { if (blockIdx.x == PW && wid == PWAVE) { u64 e_ = pt0 + 3000; while (__builtin_amdgcn_s_memrealtime() < e_) {} } } while (0)
#define CODE_LINES 64

DEVI float bl(u32 v) { return __uint_as_float(v << 16); }
DEVI float bh(u32 v) { return __uint_as_float(v & 0xffff0000u); }
DEVI u32 pk2(float a, float b) { f2 v = {a, b}; return __builtin_bit_cast(u32, __builtin_convertvector(v, bf2)); }
DEVI bf2 asbf2(u32 v) { return __builtin_bit_cast(bf2, v); }
DEVI float rbf(float a) { return bl(pk2(a, a)); }
DEVI f2 cmul(f2 x, f2 y, f2 yr) { return __builtin_elementwise_fma(x.yy, yr, x.xx * y); }
DEVI f2 rot(f2 y) { return (f2){-y.y, y.x}; }
DEVI f2 cexp(float a2, float rev, float l) {
  float e = __builtin_amdgcn_exp2f(a2 * l), p = __builtin_amdgcn_fractf(rev * l);
  return (f2){e * __builtin_amdgcn_cosf(p), e * __builtin_amdgcn_sinf(p)};
}
DEVI float sigm(float v) { return __builtin_amdgcn_rcpf(1.f + __builtin_amdgcn_exp2f(v * -1.44269504f)); }
template <class T> DEVI T lds_ld(u32 a) { return *(const LAS T*)(uintptr_t)a; }
template <class T> DEVI void lds_st(u32 a, T v) { *(LAS T*)(uintptr_t)a = v; }
template <class T> DEVI T lds_ldv(u32 a) { return *(const volatile LAS T*)(uintptr_t)a; }
template <class T> DEVI void lds_stv(u32 a, T v) { *(volatile LAS T*)(uintptr_t)a = v; }
DEVI bf4 trd(u32 a) { return __builtin_amdgcn_ds_read_tr16_b64_v4bf16((LAS bf4*)(uintptr_t)a); }
DEVI bf8 cat(bf4 a, bf4 b) { return __builtin_shufflevector(a, b, 0, 1, 2, 3, 4, 5, 6, 7); }
DEVI f4 mma(bf8 a, bf8 b, f4 c) { return __builtin_amdgcn_mfma_f32_16x16x32_bf16(a, b, c, 0, 0, 0); }
DEVI u32 rl(u32 v, int l) { return __builtin_amdgcn_readlane(v, l); }
#define DPPF(v, ctl, rm) __uint_as_float(__builtin_amdgcn_update_dpp(0u, __float_as_uint(v), ctl, rm, 0xf, true))
#define DPPU(v, ctl, rm) __builtin_amdgcn_update_dpp(0u, v, ctl, rm, 0xf, true)
DEVI u32 umax(u32 a, u32 b) { return a > b ? a : b; }
DEVI u32 umin(u32 a, u32 b) { return a < b ? a : b; }
DEVI u32 wave_umax(u32 v) {
  v = umax(v, DPPU(v, 0x128, 0xf)); v = umax(v, DPPU(v, 0x124, 0xf)); v = umax(v, DPPU(v, 0x122, 0xf)); v = umax(v, DPPU(v, 0x121, 0xf));
  v = umax(v, DPPU(v, 0x142, 0xa)); v = umax(v, DPPU(v, 0x143, 0xc));
  return rl(v, 63);
}
#define MEMF() asm volatile("" ::: "memory")

#define GLD4_SC1(d, vo, base, imm) asm volatile("global_load_dwordx4 %0, %1, %2 offset:%3 sc1" : "=v"(d) : "v"(vo), "s"(base), "i"(imm) : "memory")
#define GLD1_SC1(d, vo, base, imm) asm volatile("global_load_dword %0, %1, %2 offset:%3 sc1" : "=v"(d) : "v"(vo), "s"(base), "i"(imm) : "memory")
#define GLD2_NT(d, vo, base, imm) asm volatile("global_load_dwordx2 %0, %1, %2 offset:%3 nt" : "=v"(d) : "v"(vo), "s"(base), "i"(imm) : "memory")
#define GLD1_NT(d, vo, base) asm volatile("global_load_dword %0, %1, %2 nt" : "=v"(d) : "v"(vo), "s"(base) : "memory")
#define GLDH(d, vo, base) asm volatile("global_load_ushort %0, %1, %2" : "=v"(d) : "v"(vo), "s"(base) : "memory")
#define GLD4(d, vo, base, imm) asm volatile("global_load_dwordx4 %0, %1, %2 offset:%3" : "=v"(d) : "v"(vo), "s"(base), "i"(imm) : "memory")
DEVI void dma16(u32 vo, const void* base, u32 ldsdst) {
  u32 keep;
  asm volatile("s_mov_b32 %0, m0\n\ts_mov_b32 m0, %3\n\ts_nop 0\n\tglobal_load_lds_dwordx4 %1, %2\n\ts_mov_b32 m0, %0" : "=&s"(keep) : "v"(vo), "s"(base), "s"(ldsdst) : "memory");
}
DEVI void dma16x4(u32 v0, u32 v1, u32 v2, u32 v3, const void* base, u32 ldsdst) {
  u32 keep;
  asm volatile("s_mov_b32 %0, m0\n\ts_mov_b32 m0, %6\n\ts_nop 0\n\tglobal_load_lds_dwordx4 %1, %5\n\tglobal_load_lds_dwordx4 %2, %5 offset:1024\n\t"
               "global_load_lds_dwordx4 %3, %5 offset:2048\n\tglobal_load_lds_dwordx4 %4, %5 offset:3072\n\ts_mov_b32 m0, %0"
               : "=&s"(keep) : "v"(v0), "v"(v1), "v"(v2), "v"(v3), "s"(base), "s"(ldsdst) : "memory");
}
DEVI void dma4nt(u32 vo, const void* base, u32 ldsdst) {
  u32 keep;
  asm volatile("s_mov_b32 %0, m0\n\ts_mov_b32 m0, %3\n\ts_nop 0\n\tglobal_load_lds_dword %1, %2 nt\n\ts_mov_b32 m0, %0" : "=&s"(keep) : "v"(vo), "s"(base), "s"(ldsdst) : "memory");
}
DEVI void dma4(u32 vo, const void* base, u32 ldsdst) {
  u32 keep;
  asm volatile("s_mov_b32 %0, m0\n\ts_mov_b32 m0, %3\n\ts_nop 0\n\tglobal_load_lds_dword %1, %2\n\ts_mov_b32 m0, %0" : "=&s"(keep) : "v"(vo), "s"(base), "s"(ldsdst) : "memory");
}

DEVI bf8 band(int lane, int co, int hw) {
  int lo = (lane & 15) + co - hw - 8 * (lane >> 4);
  u32 m = (u32)(((u64)((1u << (2 * hw + 1)) - 1) << (lo + 24)) >> 24);
  u4v r;
#pragma unroll
  for (int i = 0; i < 4; i++) r[i] = ((m >> (2 * i)) & 1u) * 0x3F80u + ((m >> (2 * i + 1)) & 1u) * 0x3F800000u;
  return __builtin_bit_cast(bf8, r);
}

struct Sched {
  int n, nl, nd;
  int kind[128], p0[128], p1[128], ld[128], tile[128];
  int loff[96];
  int dsrc[40];
  int last[4];
  int lcum[129], ccum[129], yat[4];
};
constexpr int PFD = 8;
constexpr int lswap(int k, int n) { return (k ^ 1) < n ? (k ^ 1) : k; }
constexpr int tile_of(int hf, int i) { return hf == 0 ? (i == 0 ? 0 : i == 1 ? 3 : i == 2 ? 4 : 7) : (i == 0 ? 1 : i == 1 ? 2 : i == 2 ? 5 : 6); }
struct Sched2 { Sched s; int dmap[32]; };
constexpr Sched2 make_sched(int hf) {
  Sched2 r{}; Sched& s = r.s;
  for (int i = 0; i < 4; i++) {
    const int q = tile_of(hf, i), pq = i ? tile_of(hf, i - 1) : -1;
    for (int e = 0; e < 6; e++) {
      const bool n2 = (e <= q - 2) && (e >= q - 4) && !((e <= pq - 2) && (e >= pq - 4));
      const bool n3 = (e <= q - 4) && !(e <= pq - 4);
      if (!(n2 || n3)) continue;
      const int l24 = s.nl++, l16 = s.nl++;
      s.loff[l24] = 64 * (8 * e + 7) - 16; s.loff[l16] = 64 * (8 * e + 7);
      const int bsel[6] = {0, 0, 1, 2, 3, 4}, lsel[6] = {l24, l16, l16, l16, l24, l16};
      const int dm[6] = {4 * e, 4 * e + 1, 4 * e + 2, 4 * e + 3, 24 + 2 * e, 24 + 2 * e + 1};
      for (int k = 0; k < 6; k++) {
        if (k < 4 ? !n2 : !n3) continue;
        s.kind[s.n] = 1; s.p0[s.n] = bsel[k]; s.p1[s.n] = s.nd; s.ld[s.n] = lsel[k]; s.tile[s.n] = i;
        r.dmap[dm[k]] = s.nd; s.dsrc[s.nd++] = s.n; s.n++;
      }
    }
    for (int m = 0; m < 17; m++) {
      const int pp = 8 * q - m;
      if (pp < -7) continue;
      s.loff[s.nl] = 64 * (pp + 7);
      s.kind[s.n] = 0; s.p0[s.n] = m; s.ld[s.n] = s.nl++; s.tile[s.n] = i; s.n++;
    }
    for (int a = 2; a <= 4; a++)
      for (int h = 0; h < 2; h++)
        if (q - a >= 0) { s.kind[s.n] = 2; s.p0[s.n] = 2 * (q - a) + h; s.p1[s.n] = 2 * (a - 2) + h; s.ld[s.n] = -1; s.tile[s.n] = i; s.n++; }
    for (int a = 4; a <= 7; a++)
      if (q - a >= 0) { s.kind[s.n] = 2; s.p0[s.n] = 12 + (q - a); s.p1[s.n] = 6 + (a - 4); s.ld[s.n] = -1; s.tile[s.n] = i; s.n++; }
    s.last[i] = s.n - 1;
    s.yat[i] = s.n + 1;
  }
  int nl = PFD, nc = 0, mx = 0;
  s.lcum[0] = nl; s.ccum[0] = 0;
  for (int i = 0; i < s.n; i++) {
    if (s.ld[i] > mx) mx = s.ld[i];
    int slots = 2;
    if (nl < s.nl && nl <= (mx | 1) + PFD - 1) { nl++; slots--; }
    while (slots > 0 && nc < 2 * s.nd && s.dsrc[nc / 2] <= i - 2) { nc++; slots--; }
    if (slots > 0 && nl < s.nl && nl <= (mx | 1) + PFD - 1) { nl++; slots--; }
    s.lcum[i + 1] = nl; s.ccum[i + 1] = nc;
  }
  return r;
}
template <int HF> struct SchedOf { static constexpr Sched2 v = make_sched(HF); };

struct ConvRegs { bf8 An[17], T[10], bands[5]; };
template <int HF, int I>
DEVI void conv_step(u32 ub, u32 yaddr, u32 faddr, const ConvRegs& R, bf8 (&fr)[96], f4 (&D)[40], u32 (&P)[80], f4 (&acc)[4]) {
  constexpr const Sched2& S2 = SchedOf<HF>::v; constexpr const Sched& S = S2.s;
  if constexpr (I < S.n) {
    const f4 z = {0.f, 0.f, 0.f, 0.f};
    constexpr int t = S.tile[I], p0 = S.p0[I], p1 = S.p1[I], ld = S.ld[I];
    if constexpr (S.kind[I] == 0) acc[t] = mma(R.An[p0], fr[ld], p0 == 0 ? z : acc[t]);
    else if constexpr (S.kind[I] == 1) D[p1] = mma(fr[ld], R.bands[p0], z);
    else {
      constexpr int d0 = S2.dmap[2 * S.p0[I]], d1 = S2.dmap[2 * S.p0[I] + 1];
      static_assert(S.ccum[I] >= 2 * d0 + 2 && S.ccum[I] >= 2 * d1 + 2, "window not converted in time");
      acc[t] = mma(__builtin_bit_cast(bf8, ((u4v){P[2 * d0], P[2 * d0 + 1], P[2 * d1], P[2 * d1 + 1]})), R.T[p1], acc[t]);
    }
    __builtin_amdgcn_sched_barrier(0);
    constexpr int l0 = S.lcum[I], l1 = S.lcum[I + 1], c0 = S.ccum[I], c1 = S.ccum[I + 1];
    if constexpr (l0 < l1) { constexpr int k = lswap(l0, S.nl), o = S.loff[k]; fr[k] = lds_ld<bf8>(ub + o); }
    if constexpr (l0 + 1 < l1) { constexpr int k = lswap(l0 + 1, S.nl), o = S.loff[k]; fr[k] = lds_ld<bf8>(ub + o); }
    if constexpr (c0 < c1) P[c0] = pk2(D[c0 / 2][2 * (c0 & 1)], D[c0 / 2][2 * (c0 & 1) + 1]);
    if constexpr (c0 + 1 < c1) P[c0 + 1] = pk2(D[(c0 + 1) / 2][2 * ((c0 + 1) & 1)], D[(c0 + 1) / 2][2 * ((c0 + 1) & 1) + 1]);
    constexpr int yi = S.yat[0] == I ? 0 : S.yat[1] == I ? 1 : S.yat[2] == I ? 2 : I == S.n - 1 ? 3 : -1;
    if constexpr (yi >= 0) {
      const f4 y = acc[yi] + 0.0f;
      asm volatile("ds_write_b128 %0, %1 offset:%2\n\tds_write_b32 %3, %4 offset:%5" :: "v"(yaddr), "v"(y), "i"(yi * 1024), "v"(faddr), "v"(yaddr), "i"(yi * 16));
    }
    __builtin_amdgcn_sched_barrier(0);
    conv_step<HF, I + 1>(ub, yaddr, faddr, R, fr, D, P, acc);
  }
}
template <int HF, int L>
DEVI void conv_pre(u32 ub, bf8 (&fr)[96]) {
  if constexpr (L < PFD) { constexpr int k = lswap(L, 99), o = SchedOf<HF>::v.s.loff[k]; fr[k] = lds_ld<bf8>(ub + o); conv_pre<HF, L + 1>(ub, fr); }
}
template <int HF>
DEVI void conv(u32 ub, u32 yaddr, u32 faddr, const ConvRegs& R) {
  bf8 fr[96]; f4 D[40]; u32 P[80]; f4 acc[4];
  conv_pre<HF, 0>(ub, fr);
  __builtin_amdgcn_sched_barrier(0);
  conv_step<HF, 0>(ub, yaddr, faddr, R, fr, D, P, acc);
}

__global__ void __launch_bounds__(512, 2)
s4_moe_kernel(const u16* x_, const u16* Are_, const u16* Aim_, const u16* Bre_, const u16* Bim_, const u16* Cre_,
              const u16* Cim_, const u16* ldt_, const u16* dsk_, const u16* wr_, const u16* wg_, const u16* wu_,
              const u16* wd_, u16* out_, char* ws_, u64 pad_) {
  extern __shared__ __attribute__((aligned(16))) char smem[];
  const int lane = threadIdx.x & 63;
  const int wid = __builtin_amdgcn_readfirstlane(threadIdx.x >> 6);
  const int w = blockIdx.x, xc = w & 7, mm = w >> 3;
  const int cg = xc * 16 + (mm >> 1), hf = mm & 1;
  const bool prod = mm < 14;
  STAMP_DECL;
  STAMP(0);
  typedef u32 u16v __attribute__((ext_vector_type(16)));
  u16v ka, kb_;
  asm volatile("s_load_dwordx16 %0, %2, 0x0\n\ts_load_dwordx16 %1, %2, 0x40\n\ts_waitcnt lgkmcnt(0)" : "=&s"(ka), "=&s"(kb_) : "s"(__builtin_amdgcn_kernarg_segment_ptr()) : "memory");
  STAMP(14);
#define KP(T, v, i) ((T)(((u64)v[2 * (i) + 1] << 32) | v[2 * (i)]))
  const u16* x = KP(const u16*, ka, 0); const u16* Are = KP(const u16*, ka, 1); const u16* Aim = KP(const u16*, ka, 2); const u16* Bre = KP(const u16*, ka, 3);
  const u16* Bim = KP(const u16*, ka, 4); const u16* Cre = KP(const u16*, ka, 5); const u16* Cim = KP(const u16*, ka, 6); const u16* ldt = KP(const u16*, ka, 7);
  const u16* dsk = KP(const u16*, kb_, 0); const u16* wr = KP(const u16*, kb_, 1); const u16* wg = KP(const u16*, kb_, 2); const u16* wu = KP(const u16*, kb_, 3);
  const u16* wd = KP(const u16*, kb_, 4); u16* out = KP(u16*, kb_, 5); char* ws = KP(char*, kb_, 6);

  if (wid < 4) {
    __builtin_amdgcn_s_setprio(2);
    const int d = 4 * cg + wid;
    u2v xt0, xt1, xt2, xt3, xt4, xt5, xt6, xt7;
    const int R = xc * 4 + (mm >> 3), S = mm & 7;
    if (wid < 2) {
      const u32 xp = (64 * R + 8 * (lane & 7)) * 1024 + 128 * S + 64 * wid + 8 * (lane >> 3), xq = xp + 4096;
      GLD2_NT(xt0, xp, x, 0); GLD2_NT(xt1, xp, x, 1024); GLD2_NT(xt2, xp, x, 2048); GLD2_NT(xt3, xp, x, 3072);
      GLD2_NT(xt4, xq, x, 0); GLD2_NT(xt5, xq, x, 1024); GLD2_NT(xt6, xq, x, 2048); GLD2_NT(xt7, xq, x, 3072);
    }
    const u32 po = d * 256 + lane * 4;
    u32 p_ar, p_ai, p_br, p_bi, p_cr, p_ci, p_dt;
    GLDH(p_dt, 2 * d, ldt);
    GLD1_NT(p_ar, po, Are); GLD1_NT(p_ai, po, Aim); GLD1_NT(p_br, po, Bre); GLD1_NT(p_bi, po, Bim); GLD1_NT(p_cr, po, Cre); GLD1_NT(p_ci, po, Cim);
    if (wid >= 2) {
      u64 pc; asm volatile("s_getpc_b64 %0" : "=s"(pc));
      const u32 ln = (wid - 2) * 64 + lane;
      dma4((ln < CODE_LINES ? ln : 0) * 128, (const void*)pc, M_DUM);
      dma4(0u, out + 4 * cg, M_DUM);
    }
    if (lane < 32) lds_st<u4v>(UB + wid * UCH + lane * 16, (u4v){0, 0, 0, 0});
    asm volatile("s_waitcnt lgkmcnt(0)\n\ts_barrier" ::: "memory");
    STAMP(6);
    ConvRegs CR;
    CR.bands[0] = band(lane, 8, 2); CR.bands[1] = band(lane, 12, 1); CR.bands[2] = band(lane, 4, 1); CR.bands[3] = band(lane, 8, 4); CR.bands[4] = band(lane, 8, 3);
    const u32 kb = wid * KREG;
    const int hh = lane >> 5, j = lane & 31, od = lane & 1, wh = lane >> 2;
    u32 a0 = kb + 32 + 16 * j + 8 * hh;
    u32 a1 = kb + (lane < 36 ? 544 + 16 * lane : lane < 38 ? 16 * (lane - 36) : O_Y);
    u32 tA = kb + (hh ? (od ? O_TB + 32 : O_TA + 30) : (od ? O_TF + 32 : O_TE + 30)) + (j >> 1) * 2;
    u32 tB = hh ? kb + (od ? O_TD : O_TC) + 32 + (j >> 1) * 2 : tA + 64;
    constexpr u64 SAT = (u64)(O_TA + 30) | ((u64)(O_TE + 30) << 12) | ((u64)(O_TA + 94) << 24) | ((u64)(O_TC + 96) << 36) | ((u64)(O_TE + 158) << 48);
    u32 sa = kb + (lane < 20 ? (lane & 3) * (CS + 2) + (u32)((SAT >> (12 * (wh < 5 ? wh : 0))) & 0xfff) : O_Y);
    u32 zA = kb + (lane < 52 ? O_TA + lane * 16 : 1952);
    u32 cA = kb + 16 * lane, pA = lane ? cA - 8 : cA, cA2 = kb + (lane < 6 ? 1024 + 16 * lane : 1952);
    u32 m1 = lane == 32 ? 0xffffffffu : 0u, m0 = hh ? 0u : 0xffffffffu;
    u32 bn = kb + CS * (wh & 3) + 56 - 16 * (lane >> 4) + 8 * (lane & 3), bt = kb + CS * (wh & 3) - 8 * (lane >> 4) + 8 * (lane & 3);
    u32 ta = kb + lane * 4, fa = kb + j * TROW + hh * 16;
    asm volatile("" : "+v"(CR.bands[0]), "+v"(CR.bands[1]), "+v"(CR.bands[2]), "+v"(CR.bands[3]), "+v"(CR.bands[4]), "+v"(a0), "+v"(a1), "+v"(tA), "+v"(tB),
                 "+v"(sa), "+v"(zA), "+v"(cA), "+v"(pA), "+v"(cA2), "+v"(m1), "+v"(m0), "+v"(bn), "+v"(bt), "+v"(ta), "+v"(fa));
    STAMP(7);
    if (wid >= 2) {
      __builtin_amdgcn_s_sleep(SLP_W);
      const u32 row = mm * 16 + (lane >> 2);
      const u32 vo = row * 3584 + ((xc * 448) & ~127) + (lane & 3) * 128;
      dma4nt(vo, wid == 2 ? wg : wu, M_DUM);
    } else {
      u2v tg; u32 spins = 0;
      do { tg = lds_ldv<u2v>(M_TAG); } while (tg.y == 0 && ++spins < SPIN_CAP);
      STAMP(8);
      asm volatile("s_waitcnt vmcnt(7)" : "+v"(xt0), "+v"(xt1), "+v"(xt2), "+v"(xt3), "+v"(xt4), "+v"(xt5), "+v"(xt6), "+v"(xt7) :: "memory");
      STAMP(9);
      const u2v xt[8] = {xt0, xt1, xt2, xt3, xt4, xt5, xt6, xt7};
      u32 c = tg.x;
#pragma unroll
      for (int i = 0; i < 8; i++) c += xt[i].x + xt[i].y;
      const __amdgpu_buffer_rsrc_t rs = __builtin_amdgcn_make_buffer_rsrc(ws + W_XB + (R * 8 + S) * XBS, 0, XBS, 0x00020000);
      const u32 o = (8 * wid + (lane >> 3)) * 512 + (lane & 7) * 16;
#pragma unroll
      for (int i = 0; i < 4; i++)
        __builtin_amdgcn_raw_buffer_store_b128((u4v){xt[2 * i].x, xt[2 * i].y, xt[2 * i + 1].x, xt[2 * i + 1].y}, rs, o + i * 128, 0, 16);
      __builtin_amdgcn_raw_buffer_store_b32(c, rs, 8192 + wid * 256 + lane * 4, 0, 16);
    }
    asm volatile("s_cmp_lt_u32 %7, 2\n\ts_cbranch_scc1 1f\n\ts_waitcnt vmcnt(3)\n1:\n\ts_waitcnt vmcnt(5)"
                 : "+v"(p_ar), "+v"(p_ai), "+v"(p_br), "+v"(p_bi), "+v"(p_cr), "+v"(p_ci), "+v"(p_dt) : "s"(wid) : "memory", "scc");
    STAMP(1);

    const float dt = rbf(__builtin_amdgcn_exp2f(bl(p_dt) * 1.44269504f));
    f16v kd = {};
#pragma unroll 1
    for (int h = 0; h < 2; h++) {
      const u32 sh = 16 - 16 * h;
      const float a2 = dt * bh(p_ar << sh) * 1.44269504f, rv = dt * bh(p_ai << sh) * 0.159154943f;
      const f2 Bc = {dt * bh(p_br << sh), dt * bh(p_bi << sh)}, Cc = {bh(p_cr << sh), bh(p_ci << sh)};
      const f2 G = cmul(Cc, Bc, rot(Bc));
      const f2 w1 = cexp(a2, rv, 1.f), w256 = cexp(a2, rv, 256.f);
      f2 v8 = cexp(a2, -rv, 8.f);
      const f2 w1r = rot(w1), w256r = rot(w256);
      const f2 w2 = cmul(w1, w1, w1r), w2r = rot(w2), w4 = cmul(w2, w2, w2r), w4r = rot(w4);
      const f2 v16 = cmul(v8, v8, rot(v8)), v16r = rot(v16);
#define TW(r, v) lds_st<u32>(ta + (r) * TROW, pk2((v).x, (v).y))
      f2 fz = G, ge = v16, go = v8, fh = cmul(G, w256, w256r);
      TW(0, fz); TW(21, ((f2){1.f, 0.f})); TW(22, go); TW(23, ge); TW(8, fh);
      f2 t2 = cmul(fh, w2, w2r); TW(9, t2); t2 = cmul(t2, w2, w2r); TW(10, t2); t2 = cmul(t2, w2, w2r); TW(11, t2);
#pragma unroll
      for (int i = 1; i < 16; i++) {
        go = cmul(go, v16, v16r); TW(22 + 2 * i, go);
        if (i < 15) { ge = cmul(ge, v16, v16r); TW(23 + 2 * i, ge); }
        if (i < 8) { fz = cmul(fz, w1, w1r); TW(i, fz); }
        else {
          fh = cmul(fh, w256, w256r);
          if (i < 10) { TW(12 + (i - 8), fh); f2 t4 = cmul(fh, w4, w4r); TW(14 + (i - 8), t4); }
          else if (i < 15) TW(16 + (i - 10), fh);
        }
      }
      bf8 tf[16];
#pragma unroll
      for (int s = 0; s < 8; s++) { tf[2 * s] = lds_ld<bf8>(fa + 32 * s); tf[2 * s + 1] = lds_ld<bf8>(fa + 21 * TROW + 32 * s); }
      __builtin_amdgcn_sched_barrier(0);
#pragma unroll
      for (int s = 0; s < 8; s++) kd = __builtin_amdgcn_mfma_f32_32x32x16_bf16(tf[2 * s], tf[2 * s + 1], kd, 0, 0, 0);
    }
#pragma unroll
    for (int q = 0; q < 4; q++) lds_st<u4v>(zA + q * CS, (u4v){0, 0, 0, 0});
    STAMP(2);
    {
      lds_st<u2v>(a0, (u2v){pk2(kd[0], kd[1]), pk2(kd[2], kd[3])});
      const float en = DPPF(kd[4], 0x130, 0xf);
      const u32 e00 = pk2(kd[4], kd[4]);
      lds_st<u4v>(a1, (u4v){e00 & (m0 | m1), (pk2(kd[5], kd[6]) & m0) | (e00 & m1 & 0xffffu), pk2(kd[6], kd[6]) & m0, pk2(kd[7], en) & m0});
      const u4v c = lds_ld<u4v>(cA), c2 = lds_ld<u4v>(cA2);
      const u2v p = lds_ld<u2v>(pA), p2 = lds_ld<u2v>(cA2 - 8);
      const u32 v01 = hh ? pk2(kd[4], kd[5]) : pk2(kd[8], kd[9]), v23 = hh ? pk2(kd[6], kd[7]) : pk2(kd[10], kd[11]);
#pragma unroll
      for (int q = 0; q < 4; q++) {
        lds_st<u16>(tA + q * (CS + 2), (u16)v01); lds_st<u16>(tA + q * (CS + 2) + 32, (u16)(v01 >> 16));
        lds_st<u16>(tB + q * (CS + 2), (u16)v23); lds_st<u16>(tB + q * (CS + 2) + 32, (u16)(v23 >> 16));
      }
      const u32 k8 = pk2(kd[8], kd[8]);
      const u32 al = rl(k8, 0), k2048 = rl(k8, 32);
      lds_st<u16>(sa, (u16)(lane < 8 ? 0u : lane < 16 ? al : k2048));
      lds_st<u32>(M_AL + wid * 4, al << 16);
#define SHIFTS(c, p, A) do { \
      const u32 s0 = __builtin_amdgcn_alignbit(p.y, p.x, 16), s1 = __builtin_amdgcn_alignbit(c.x, p.y, 16), s2 = __builtin_amdgcn_alignbit(c.y, c.x, 16), \
                s3 = __builtin_amdgcn_alignbit(c.z, c.y, 16), s4 = __builtin_amdgcn_alignbit(c.w, c.z, 16); \
      lds_st<u4v>(A + CS, (u4v){s1, s2, s3, s4}); lds_st<u4v>(A + 2 * CS, (u4v){p.y, c.x, c.y, c.z}); lds_st<u4v>(A + 3 * CS, (u4v){s0, s1, s2, s3}); } while (0)
      SHIFTS(c, p, cA);
      SHIFTS(c2, p2, cA2);
    }
    MEMF();
#pragma unroll
    for (int a = 2; a <= 4; a++) {
      CR.T[2 * (a - 2)] = cat(trd(bt + O_TA + 32 * a - 32), trd(bt + O_TB + 32 * a - 32));
      CR.T[2 * (a - 2) + 1] = cat(trd(bt + O_TC + 32 * a - 32), trd(bt + O_TD + 32 * a - 32));
    }
#pragma unroll
    for (int a = 4; a <= 7; a++) CR.T[6 + a - 4] = cat(trd(bt + O_TE + 32 * a - 96), trd(bt + O_TF + 32 * a - 96));
#pragma unroll
    for (int m = 0; m < 17; m++) CR.An[m] = cat(trd(bn + 64 * m + 8), trd(bn + 64 * m));
    u32 ub = UB + wid * UCH + 32 + 32 * (lane & 15) + 16 * (lane >> 4);
    u32 ya = kb + O_Y + 64 * (lane & 15) + 16 * (lane >> 4), fl = M_YF + wid * 4;
    asm volatile("" : "+v"(ub), "+v"(ya), "+v"(fl));
    STAMP(3);
    asm volatile("s_waitcnt lgkmcnt(0)\n\ts_barrier" ::: "memory");
    STAMP(4);
    if (hf == 0) conv<0>(ub, ya, fl, CR); else conv<1>(ub, ya, fl, CR);
    STAMP(5);
    asm volatile("s_waitcnt vmcnt(0) lgkmcnt(0)" ::: "memory");
    STAMP_FLUSH();
    return;
  }

  const int cw = wid - 4;
  const int sp = xc * 32 + mm;
  u32 ep = 0;
  if (cw == 0) ep = __hip_atomic_load((GAS u32*)(ws + W_EP), __ATOMIC_RELAXED, __HIP_MEMORY_SCOPE_AGENT);
  const u4v xr0 = __builtin_nontemporal_load((const GAS u4v*)(x + (size_t)(8 * sp + 2 * cw) * 512) + lane);
  const u4v xr1 = __builtin_nontemporal_load((const GAS u4v*)(x + (size_t)(8 * sp + 2 * cw + 1) * 512) + lane);
  const u4v wv = ((const GAS u4v*)wr)[lane];
  const u2v dk = *(const GAS u2v*)(dsk + 4 * cg);
  if (cw == 3) lds_st<u32>(MB + lane * 4, 0u);
  asm volatile("s_waitcnt lgkmcnt(0)\n\ts_barrier" ::: "memory");
  u32 tag;
  if (cw == 0) {
    tag = __builtin_amdgcn_readfirstlane(ep) + 1u;
    if (lane == 0) lds_stv<u2v>(M_TAG, (u2v){tag, 1u});
  }
  {
    const bf2 ones = __builtin_bit_cast(bf2, 0x3F803F80u);
    float s0 = 0, q0 = 0, d0 = 0, s1 = 0, q1 = 0, d1 = 0, sw = 0;
#pragma unroll
    for (int i = 0; i < 4; i++) {
      const bf2 a = asbf2(xr0[i]), b = asbf2(xr1[i]), c = asbf2(wv[i]);
      s0 = __builtin_amdgcn_fdot2_f32_bf16(a, ones, s0, false); q0 = __builtin_amdgcn_fdot2_f32_bf16(a, a, q0, false); d0 = __builtin_amdgcn_fdot2_f32_bf16(a, c, d0, false);
      s1 = __builtin_amdgcn_fdot2_f32_bf16(b, ones, s1, false); q1 = __builtin_amdgcn_fdot2_f32_bf16(b, b, q1, false); d1 = __builtin_amdgcn_fdot2_f32_bf16(b, c, d1, false);
      sw = __builtin_amdgcn_fdot2_f32_bf16(c, ones, sw, false);
    }
    auto sw32 = [](float a, float b) { auto r = __builtin_amdgcn_permlane32_swap(__float_as_uint(a), __float_as_uint(b), false, false); return __uint_as_float(r[0]) + __uint_as_float(r[1]); };
    float s = sw32(s0, s1), q = sw32(q0, q1), dd = sw32(d0, d1); sw = sw32(sw, sw);
#define RED(v) v += DPPF(v, 0x128, 0xf);
    s += DPPF(s, 0x128, 0xf); q += DPPF(q, 0x128, 0xf); dd += DPPF(dd, 0x128, 0xf); sw += DPPF(sw, 0x128, 0xf);
    s += DPPF(s, 0x124, 0xf); q += DPPF(q, 0x124, 0xf); dd += DPPF(dd, 0x124, 0xf); sw += DPPF(sw, 0x124, 0xf);
    s += DPPF(s, 0x122, 0xf); q += DPPF(q, 0x122, 0xf); dd += DPPF(dd, 0x122, 0xf); sw += DPPF(sw, 0x122, 0xf);
    s += DPPF(s, 0x121, 0xf); q += DPPF(q, 0x121, 0xf); dd += DPPF(dd, 0x121, 0xf); sw += DPPF(sw, 0x121, 0xf);
    s += DPPF(s, 0x142, 0xa); q += DPPF(q, 0x142, 0xa); dd += DPPF(dd, 0x142, 0xa); sw += DPPF(sw, 0x142, 0xa);
    const float mean = s * (1.f / 512.f);
    const float var = fmaxf(q * (1.f / 512.f) - mean * mean, 0.f);
    const float mb = rbf(mean), sb = rbf(__builtin_amdgcn_sqrtf(rbf(rbf(var) + 1e-5f)));
    const float r = (dd - mb * sw) * __builtin_amdgcn_rcpf(sb);
    const u32 ms = pk2(mb, sb), rb = pk2(r, r) & 0xffffu;
    const u32 ms0 = rl(ms, 31), ms1 = rl(ms, 63), rr = rl(rb, 31) | (rl(rb, 63) << 16);
    if (cw != 0) {
      u2v tg; u32 spins = 0;
      do { tg = lds_ldv<u2v>(M_TAG); } while (tg.y == 0 && ++spins < SPIN_CAP);
      tag = __builtin_amdgcn_readfirstlane(tg.x);
    }
    const __amdgpu_buffer_rsrc_t rs = __builtin_amdgcn_make_buffer_rsrc(ws + W_ST, 0, 16384, 0x00020000);
    if (lane == 0) __builtin_amdgcn_raw_buffer_store_b128((u4v){ms0, ms1, rr, ms0 + ms1 + rr + tag + tag - 1u}, rs, cw * 4096 + sp * 16, 0, 16);
  }
  STAMP(1);
  const u32 chs = tag + tag - 1u;
  u4v su0, su1, su2, su3, xu0, xu1, xu2, xu3; u32 xk;
  const u32 so = W_ST + (64 * cw + lane) * 16;
  const u32 xb = W_XB + ((8 * cw + (lane >> 3)) * 8 + xc) * XBS;
  const u32 xo = xb + (mm >> 1) * 512 + (lane & 7) * 16, xko = xb + 8192 + (mm >> 1) * 32 + (lane & 7) * 4;
  const char* ws1 = ws + 4096; const char* ws2 = ws + 8192; const char* ws3 = ws + 12288;
#define LOAD_S() do { GLD4_SC1(su0, so, ws, 0); GLD4_SC1(su1, so, ws1, 0); GLD4_SC1(su2, so, ws2, 0); GLD4_SC1(su3, so, ws3, 0); } while (0)
#define LOAD_X() do { GLD4_SC1(xu0, xo, ws, 0); GLD4_SC1(xu1, xo, ws, 128); GLD4_SC1(xu2, xo, ws, 256); GLD4_SC1(xu3, xo, ws, 384); GLD1_SC1(xk, xko, ws, 0); } while (0)
#define OK_S() ((((su0.x + su0.y + su0.z + chs) ^ su0.w) | ((su1.x + su1.y + su1.z + chs) ^ su1.w) | ((su2.x + su2.y + su2.z + chs) ^ su2.w) | ((su3.x + su3.y + su3.z + chs) ^ su3.w)) == 0)
#define SUM4(v) (v.x + v.y + v.z + v.w)
#define OK_X() (SUM4(xu0) + SUM4(xu1) + SUM4(xu2) + SUM4(xu3) + tag == xk)
  __builtin_amdgcn_s_sleep(SLP_S);
  LOAD_S();
  __builtin_amdgcn_s_sleep(SLP_X);
  LOAD_X();
  const u32 fb = FB + cw * 12288;
  const int fp = xc * 14 + mm;
#define PULL() do { \
    const u32 go = (128 * cw + (lane >> 1)) * 3584 + fp * 32 + (lane & 1) * 16; \
    constexpr u32 GS = 32 * 3584 - 1024; \
    dma16x4(go, go + GS, go + 2 * GS, go + 3 * GS, wg, fb); \
    dma16x4(go, go + GS, go + 2 * GS, go + 3 * GS, wu, fb + 4096); \
    const u32 dof = (16 * fp + (lane >> 4)) * 1024 + 256 * cw + (lane & 15) * 16; \
    dma16x4(dof, dof + 3072, dof + 6144, dof + 9216, wd, fb + 8192); } while (0)
  asm volatile("s_waitcnt vmcnt(5)" : "+v"(su0), "+v"(su1), "+v"(su2), "+v"(su3) :: "memory");
  STAMP(2);
  for (u32 spins = 0; !__all(OK_S()) && spins < SPIN_CAP; spins++) {
    asm volatile("s_sleep 2\n\tglobal_load_dwordx4 %0, %4, %5 sc1\n\tglobal_load_dwordx4 %1, %4, %6 sc1\n\tglobal_load_dwordx4 %2, %4, %7 sc1\n\t"
                 "global_load_dwordx4 %3, %4, %8 sc1\n\ts_waitcnt vmcnt(0)"
                 : "=&v"(su0), "=&v"(su1), "=&v"(su2), "=&v"(su3) : "v"(so), "s"(ws), "s"(ws1), "s"(ws2), "s"(ws3) : "memory");
  }
  STAMP(3);
  u32 k1, k2;
  {
    const u32 ib = 0xffffu - (512 * cw + 8 * lane);
    auto key = [&](u32 b, int i) { u32 o = b ^ ((u32)((int)b >> 31) | 0x80000000u); return (o & 0xffff0000u) | (ib - i); };
    k1 = key(su0.z << 16, 0); k2 = 0;
    auto ins = [&](u32 k) { k2 = umax(k2, umin(k1, k)); k1 = umax(k1, k); };
    ins(key(su0.z & 0xffff0000u, 1)); ins(key(su1.z << 16, 2)); ins(key(su1.z & 0xffff0000u, 3)); ins(key(su2.z << 16, 4));
    ins(key(su2.z & 0xffff0000u, 5)); ins(key(su3.z << 16, 6)); ins(key(su3.z & 0xffff0000u, 7));
    const u32 K1 = wave_umax(k1), K2 = wave_umax(k1 == K1 ? k2 : k1);
    if (lane == 0) lds_stv<u2v>(M_TOP + cw * 8, (u2v){K1, K2});
    u4v ta, tb; u32 spins = 0;
    do { ta = lds_ldv<u4v>(M_TOP); tb = lds_ldv<u4v>(M_TOP + 16); } while ((ta.x == 0 || ta.z == 0 || tb.x == 0 || tb.z == 0) && ++spins < SPIN_CAP);
    const u32 a1 = umax(ta.x, ta.z), a2 = umax(umin(ta.x, ta.z), umax(ta.y, ta.w)), b1 = umax(tb.x, tb.z), b2 = umax(umin(tb.x, tb.z), umax(tb.y, tb.w));
    k1 = __builtin_amdgcn_readfirstlane(umax(a1, b1)); k2 = __builtin_amdgcn_readfirstlane(umax(umin(a1, b1), umax(a2, b2)));
  }
  const int t0 = (0xffffu - (k1 & 0xffffu)) & 2047, t1 = (0xffffu - (k2 & 0xffffu)) & 2047;
  u4v xs0, xs1, xs2, xs3;
  if (prod) {
    const u32 xso = ((lane & 1) ? t1 : t0) * 1024 + 256 * cw + 16 * (lane >> 4);
    GLD4(xs0, xso, x, 0); GLD4(xs1, xso, x, 64); GLD4(xs2, xso, x, 128); GLD4(xs3, xso, x, 192);
  }
  if (w == 0 && cw == 0 && lane == 0) __hip_atomic_store((GAS u32*)(ws + W_EP), tag, __ATOMIC_RELAXED, __HIP_MEMORY_SCOPE_AGENT);
  if ((t0 >> 3) == 64 * cw + lane) { lds_st<u4v>(M_SEL, (u4v){su0.x, su0.y, su1.x, su1.y}); lds_st<u4v>(M_SEL + 16, (u4v){su2.x, su2.y, su3.x, su3.y}); }
  if ((t1 >> 3) == 64 * cw + lane) { lds_st<u4v>(M_SEL + 32, (u4v){su0.x, su0.y, su1.x, su1.y}); lds_st<u4v>(M_SEL + 48, (u4v){su2.x, su2.y, su3.x, su3.y}); }
  float na[8], nb[8];
  {
    const u32 msv[8] = {su0.x, su0.y, su1.x, su1.y, su2.x, su2.y, su3.x, su3.y};
#pragma unroll
    for (int i = 0; i < 8; i++) { na[i] = __builtin_amdgcn_rcpf(bh(msv[i])); nb[i] = -bl(msv[i]) * na[i]; }
  }
  asm volatile("" : "+v"(na[0]), "+v"(na[1]), "+v"(na[2]), "+v"(na[3]), "+v"(na[4]), "+v"(na[5]), "+v"(na[6]), "+v"(na[7]),
               "+v"(nb[0]), "+v"(nb[1]), "+v"(nb[2]), "+v"(nb[3]), "+v"(nb[4]), "+v"(nb[5]), "+v"(nb[6]), "+v"(nb[7]));
  STAMP(4);
#define XW "4"
  asm volatile("s_cmp_lt_u32 %5, 111\n\ts_cbranch_scc1 1f\n\ts_waitcnt vmcnt(0)\n1:\n\ts_waitcnt vmcnt(" XW ")"
               : "+v"(xu0), "+v"(xu1), "+v"(xu2), "+v"(xu3), "+v"(xk) : "s"(w - 1) : "memory", "scc");
  for (u32 spins = 0; !__all(OK_X()) && spins < SPIN_CAP; spins++) {
    asm volatile("s_sleep 1\n\tglobal_load_dwordx4 %0, %5, %7 sc1\n\tglobal_load_dwordx4 %1, %5, %7 offset:128 sc1\n\tglobal_load_dwordx4 %2, %5, %7 offset:256 sc1\n\t"
                 "global_load_dwordx4 %3, %5, %7 offset:384 sc1\n\tglobal_load_dword %4, %6, %7 sc1\n\ts_waitcnt vmcnt(0)"
                 : "=&v"(xu0), "=&v"(xu1), "=&v"(xu2), "=&v"(xu3), "=&v"(xk) : "v"(xo), "v"(xko), "s"(ws) : "memory");
  }
  STAMP(5);
  u32 xa[8] = {xu0.x, xu0.z, xu1.x, xu1.z, xu2.x, xu2.z, xu3.x, xu3.z};
  u32 xcv[8] = {xu0.y, xu0.w, xu1.y, xu1.w, xu2.y, xu2.w, xu3.y, xu3.w};
  {
    u4v c0, c1, c2, c3;
#pragma unroll
    for (int i = 0; i < 4; i++) {
      const int r0 = 2 * i, r1 = 2 * i + 1;
      const f2 p0 = __builtin_elementwise_fma((f2){bl(xa[r0]), bh(xa[r0])}, (f2){na[r0], na[r0]}, (f2){nb[r0], nb[r0]});
      const f2 p1 = __builtin_elementwise_fma((f2){bl(xa[r1]), bh(xa[r1])}, (f2){na[r1], na[r1]}, (f2){nb[r1], nb[r1]});
      const f2 p2 = __builtin_elementwise_fma((f2){bl(xcv[r0]), bh(xcv[r0])}, (f2){na[r0], na[r0]}, (f2){nb[r0], nb[r0]});
      const f2 p3 = __builtin_elementwise_fma((f2){bl(xcv[r1]), bh(xcv[r1])}, (f2){na[r1], na[r1]}, (f2){nb[r1], nb[r1]});
      c0[i] = pk2(p0.x, p1.x); c1[i] = pk2(p0.y, p1.y); c2[i] = pk2(p2.x, p3.x); c3[i] = pk2(p2.y, p3.y);
    }
    const u32 ua = UB + 512 + (64 * cw + lane) * 16;
    lds_st<u4v>(ua, c0); lds_st<u4v>(ua + UCH, c1); lds_st<u4v>(ua + 2 * UCH, c2); lds_st<u4v>(ua + 3 * UCH, c3);
  }
  asm volatile("s_waitcnt lgkmcnt(0)\n\ts_barrier" ::: "memory");
  STAMP(6);

  if (prod) {
    PULL();
    const u32 msel = lds_ld<u32>(M_SEL + (lane & 1) * 32 + (((lane & 1) ? t1 : t0) & 7) * 4);
    const float a = __builtin_amdgcn_rcpf(bh(msel)), b = -bl(msel) * a;
    asm volatile("s_waitcnt vmcnt(0)" : "+v"(xs0), "+v"(xs1), "+v"(xs2), "+v"(xs3) :: "memory");
    STAMP(7);
    auto nrm = [&](u4v v) { u4v r;
#pragma unroll
      for (int i = 0; i < 4; i++) r[i] = pk2(fmaf(bl(v[i]), a, b), fmaf(bh(v[i]), a, b));
      return __builtin_bit_cast(bf8, r); };
    const bf8 xf[4] = {nrm(xs0), nrm(xs1), nrm(xs2), nrm(xs3)};
    const int g = lane >> 4, q = (lane >> 2) & 3, p = lane & 3;
    const u32 ga = fb + (8 * g + q) * 32 + 8 * p;
    f4 ag = {0, 0, 0, 0}, au = {0, 0, 0, 0};
#pragma unroll
    for (int s = 0; s < 4; s++) {
      ag = mma(cat(trd(ga + s * 1024), trd(ga + s * 1024 + 128)), xf[s], ag);
      au = mma(cat(trd(ga + 4096 + s * 1024), trd(ga + 4096 + s * 1024 + 128)), xf[s], au);
    }
    if ((lane & 15) < 2) {
      lds_st<f4>(M_PART + cw * 256 + (lane & 1) * 64 + g * 16, ag);
      lds_st<f4>(M_PART + cw * 256 + 128 + (lane & 1) * 64 + g * 16, au);
    }
    MEMF();
    if (lane == 0) lds_stv<u32>(M_FF + cw * 4, 1u);
    u4v ff; u32 spins = 0;
    do { ff = lds_ldv<u4v>(M_FF); } while ((ff.x & ff.y & ff.z & ff.w) == 0 && ++spins < SPIN_CAP);
    MEMF();
    f4 sg = {0, 0, 0, 0}, su = {0, 0, 0, 0};
#pragma unroll
    for (int v = 0; v < 4; v++) {
      sg += lds_ld<f4>(M_PART + v * 256 + (lane & 1) * 64 + g * 16);
      su += lds_ld<f4>(M_PART + v * 256 + 128 + (lane & 1) * 64 + g * 16);
    }
    float hv[4];
#pragma unroll
    for (int i = 0; i < 4; i++) { const float gg = rbf(sg[i]); hv[i] = rbf(gg * sigm(gg)) * rbf(su[i]); }
    const s4v hb = __builtin_bit_cast(s4v, ((u2v){pk2(hv[0], hv[1]), pk2(hv[2], hv[3])}));
    const u32 da = fb + 8192 + g * 1024 + q * 256 + 8 * p;
    const __amdgpu_buffer_rsrc_t rs = __builtin_amdgcn_make_buffer_rsrc(ws + W_FB + fp * 8192, 0, 8192, 0x00020000);
    const u32 fo = (32 * cw + g) * 64 + (lane & 1) * 32 + (lane & 2) * 8;
#pragma unroll
    for (int t = 0; t < 8; t++) {
      const f4 y = __builtin_amdgcn_mfma_f32_16x16x16bf16_1k(__builtin_bit_cast(s4v, trd(da + 32 * t)), hb, (f4){0, 0, 0, 0}, 0, 0, 0);
      const u4v o = {__float_as_uint((lane & 2) ? y[2] : y[0]), tag, __float_as_uint((lane & 2) ? y[3] : y[1]), tag};
      if ((lane & 12) == 0) __builtin_amdgcn_raw_buffer_store_b128(o, rs, fo + t * 256, 0, 16);
    }
    STAMP(8);
  }

  const int up = (cw & 1) ^ hf;
  const int tile = 2 * cw + up;
  const int tl = 8 * (lane & 31) + 4 * (lane >> 5), tr0 = 256 * tile + tl;
  float xv[4][4], uv[4][4], pre[4][4], gt[4][4];
#pragma unroll
  for (int r = 0; r < 4; r++) {
    auto s0 = __builtin_amdgcn_permlane32_swap(xa[r], xa[r + 4], false, false);
    auto s1 = __builtin_amdgcn_permlane32_swap(xcv[r], xcv[r + 4], false, false);
    const u32 e0 = up ? s0[1] : s0[0], e1 = up ? s1[1] : s1[0];
    xv[0][r] = bl(e0); xv[1][r] = bh(e0); xv[2][r] = bl(e1); xv[3][r] = bh(e1);
  }
  {
    const f4 alv = lds_ld<f4>(M_AL);
    const float dv[4] = {bl(dk.x), bh(dk.x), bl(dk.y), bh(dk.y)};
    const u32 ua = UB + 512 + tr0 * 2;
    const u32 ca = tile >= 4 ? ua - 2064 : UB;
#pragma unroll
    for (int c = 0; c < 4; c++) {
      const u2v a = lds_ld<u2v>(ua + c * UCH), p = lds_ld<u2v>(ca + c * UCH), n = lds_ld<u2v>(ca + c * UCH + 8);
      uv[c][0] = bl(a.x); uv[c][1] = bh(a.x); uv[c][2] = bl(a.y); uv[c][3] = bh(a.y);
      pre[c][0] = uv[c][0] * dv[c] - alv[c] * bh(p.y); pre[c][1] = uv[c][1] * dv[c] - alv[c] * bl(n.x);
      pre[c][2] = uv[c][2] * dv[c] - alv[c] * bh(n.x); pre[c][3] = uv[c][3] * dv[c] - alv[c] * bl(n.y);
#pragma unroll
      for (int r = 0; r < 4; r++) gt[c][r] = uv[c][r] * sigm(uv[c][r]);
    }
  }
  STAMP(9);
  {
    u4v yf; u32 spins = 0;
    do { __builtin_amdgcn_s_sleep(1); yf = lds_ldv<u4v>(M_YF + cw * 16); } while (umin(umin(yf.x, yf.y), umin(yf.z, yf.w)) == 0 && ++spins < SPIN_CAP);
  }
  MEMF();
  STAMP(10);
#pragma unroll
  for (int c = 0; c < 4; c++) {
    const f4 y = lds_ld<f4>(c * KREG + O_Y + cw * 1024 + tl * 4);
#pragma unroll
    for (int r = 0; r < 4; r++) pre[c][r] += y[r];
  }
  const bool late = (tr0 >> 2) == (t0 >> 2) || (tr0 >> 2) == (t1 >> 2);
  char* op = (char*)out + (size_t)tr0 * 1024 + cg * 8;
  if (!late) {
#pragma unroll
    for (int r = 0; r < 4; r++)
      *(GAS u2v*)(op + r * 1024) = (u2v){pk2(fmaf(pre[0][r], gt[0][r], xv[0][r]), fmaf(pre[1][r], gt[1][r], xv[1][r])),
                                         pk2(fmaf(pre[2][r], gt[2][r], xv[2][r]), fmaf(pre[3][r], gt[3][r], xv[3][r]))};
  }
  STAMP(11);
  if ((t0 >> 8) == tile || (t1 >> 8) == tile) {
#pragma unroll 1
    for (int k = 0; k < 2; k++) {
      const int tk = k ? t1 : t0; const u32 kk = k ? k2 : k1;
      if ((tk >> 8) != tile) continue;
      const u32 fo = W_FB + (lane >> 1) * 8192 + cg * 64 + k * 32 + (lane & 1) * 16;
      const u32 fo3 = lane < 32 ? fo + 96 * 8192 : fo;
      u4v g0, g1, g2, g3;
      for (u32 spins = 0; spins < SPIN_CAP; spins++) {
        asm volatile("global_load_dwordx4 %0, %4, %8 sc1\n\tglobal_load_dwordx4 %1, %5, %8 sc1\n\tglobal_load_dwordx4 %2, %6, %8 sc1\n\t"
                     "global_load_dwordx4 %3, %7, %8 sc1\n\ts_waitcnt vmcnt(0)"
                     : "=&v"(g0), "=&v"(g1), "=&v"(g2), "=&v"(g3) : "v"(fo), "v"(fo + 32 * 8192), "v"(fo + 64 * 8192), "v"(fo3), "s"(ws) : "memory");
        if (__all(((g0.y ^ tag) | (g0.w ^ tag) | (g1.y ^ tag) | (g1.w ^ tag) | (g2.y ^ tag) | (g2.w ^ tag) | (g3.y ^ tag) | (g3.w ^ tag)) == 0)) break;
        __builtin_amdgcn_s_sleep(1);
      }
      float v0 = __uint_as_float(g0.x) + __uint_as_float(g1.x) + __uint_as_float(g2.x) + (lane < 32 ? __uint_as_float(g3.x) : 0.f);
      float v1 = __uint_as_float(g0.z) + __uint_as_float(g1.z) + __uint_as_float(g2.z) + (lane < 32 ? __uint_as_float(g3.z) : 0.f);
      v0 += DPPF(v0, 0x128, 0xf); v1 += DPPF(v1, 0x128, 0xf); v0 += DPPF(v0, 0x124, 0xf); v1 += DPPF(v1, 0x124, 0xf);
      v0 += DPPF(v0, 0x122, 0xf); v1 += DPPF(v1, 0x122, 0xf);
      float yv[4];
      {
        const u32 a = __float_as_uint(v0), b = __float_as_uint(v1);
        yv[0] = (__uint_as_float(rl(a, 0)) + __uint_as_float(rl(a, 16))) + (__uint_as_float(rl(a, 32)) + __uint_as_float(rl(a, 48)));
        yv[1] = (__uint_as_float(rl(b, 0)) + __uint_as_float(rl(b, 16))) + (__uint_as_float(rl(b, 32)) + __uint_as_float(rl(b, 48)));
        yv[2] = (__uint_as_float(rl(a, 1)) + __uint_as_float(rl(a, 17))) + (__uint_as_float(rl(a, 33)) + __uint_as_float(rl(a, 49)));
        yv[3] = (__uint_as_float(rl(b, 1)) + __uint_as_float(rl(b, 17))) + (__uint_as_float(rl(b, 33)) + __uint_as_float(rl(b, 49)));
      }
      const u32 ob = kk & 0xffff0000u;
      const float rs = rbf(sigm(__uint_as_float((ob & 0x80000000u) ? (ob ^ 0x80000000u) : (~ob & 0xffff0000u))));
#pragma unroll
      for (int c = 0; c < 4; c++) {
        const float upd = rbf(rbf(yv[c]) * rs);
#pragma unroll
        for (int r = 0; r < 4; r++) {
          const float b = rbf(uv[c][r] + upd);
          gt[c][r] = (tr0 + r == tk) ? b * sigm(b) : gt[c][r];
        }
      }
    }
    if (late) {
#pragma unroll
      for (int r = 0; r < 4; r++)
        *(GAS u2v*)(op + r * 1024) = (u2v){pk2(fmaf(pre[0][r], gt[0][r], xv[0][r]), fmaf(pre[1][r], gt[1][r], xv[1][r])),
                                           pk2(fmaf(pre[2][r], gt[2][r], xv[2][r]), fmaf(pre[3][r], gt[3][r], xv[3][r]))};
    }
    STAMP(12);
  }
  asm volatile("s_waitcnt vmcnt(0)" ::: "memory");
  STAMP(13);
  STAMP_FLUSH();
}

extern "C" void kernel_launch(void* const* d_in, const int* in_sizes, int n_in, void* d_out, int out_size,
                              void* d_ws, size_t ws_size, hipStream_t stream) {
  (void)hipFuncSetAttribute((const void*)s4_moe_kernel, hipFuncAttributeMaxDynamicSharedMemorySize, LDS_BYTES);
  s4_moe_kernel<<<256, 512, LDS_BYTES, stream>>>(
      (const u16*)d_in[0], (const u16*)d_in[1], (const u16*)d_in[2], (const u16*)d_in[3], (const u16*)d_in[4],
      (const u16*)d_in[5], (const u16*)d_in[6], (const u16*)d_in[7], (const u16*)d_in[8], (const u16*)d_in[10],
      (const u16*)d_in[11], (const u16*)d_in[12], (const u16*)d_in[13], (u16*)d_out, (char*)d_ws, 0ull);
}
```

```cpp
#define KMODE 0
#define PROBE 99
#define PW 200
#define PWAVE 4
#define PULL_LATE
#include <hip/hip_runtime.h>
#include <hip/hip_bf16.h>
#include <stdint.h>

typedef unsigned short u16;
typedef unsigned int u32;
typedef unsigned long long u64;
typedef float f2 __attribute__((ext_vector_type(2)));
typedef float f4 __attribute__((ext_vector_type(4)));
typedef float f16v __attribute__((ext_vector_type(16)));
typedef u32 u2v __attribute__((ext_vector_type(2)));
typedef u32 u4v __attribute__((ext_vector_type(4)));
typedef __bf16 bf2 __attribute__((ext_vector_type(2)));
typedef __bf16 bf4 __attribute__((ext_vector_type(4)));
typedef __bf16 bf8 __attribute__((ext_vector_type(8)));
typedef short s4v __attribute__((ext_vector_type(4)));
#define LAS __attribute__((address_space(3)))
#define GAS __attribute__((address_space(1)))
#define DEVI __device__ __forceinline__

#define SLP_S 10
#define SLP_X 4
#define SLP_W 20
#define SPIN_CAP (1u << 18)

constexpr int KREG = 14592;
constexpr int TROW = 272;
constexpr int CS = 2112;
constexpr int O_TA = 1120, O_TB = 1248, O_TC = 1376, O_TD = 1504, O_TE = 1632, O_TF = 1792;
constexpr int O_Y = 8448;
constexpr int UB = 4 * KREG;
constexpr int UCH = 4608;
constexpr int FB = UB + 4 * UCH;
constexpr int MB = FB + 49152;
constexpr int M_TAG = MB;
constexpr int M_TOP = MB + 16;
constexpr int M_YF = MB + 64;
constexpr int M_FF = MB + 128;
constexpr int M_AL = MB + 144;
constexpr int M_SEL = MB + 192;
constexpr int M_PART = MB + 256;
constexpr int M_DUM = MB + 1280;
constexpr int LDS_BYTES = MB + 1536;

constexpr u32 W_EP = 0;
constexpr u32 W_ST = 4096;
constexpr u32 W_XB = 65536;
constexpr u32 XBS = 8704;
constexpr u32 W_FB = 4u << 20;
constexpr u32 W_STAMP = 8u << 20;

#undef STAMPS
#define STAMP(i) do { if ((i) == 0) asm volatile("s_memrealtime %0\n\ts_waitcnt lgkmcnt(0)" : "=s"(pt0) :: "memory"); if ((i) == PROBE) asm volatile("s_memrealtime %0\n\ts_waitcnt lgkmcnt(0)" : "=s"(ptk) :: "memory"); } while (0)
#define STAMP_DECL u64 pt0 = 0, ptk = 0
#define STAMP_FLUSH() do { if (blockIdx.x == PW && wid == PWAVE) { u64 e_ = pt0 + 3000; while (__builtin_amdgcn_s_memrealtime() < e_) {} if (lane == 0) *(GAS u64*)(ws + W_STAMP) = __builtin_amdgcn_s_memrealtime(); } } while (0)
#define CODE_LINES 64

DEVI float bl(u32 v) { return __uint_as_float(v << 16); }
DEVI float bh(u32 v) { return __uint_as_float(v & 0xffff0000u); }
DEVI u32 pk2(float a, float b) { f2 v = {a, b}; return __builtin_bit_cast(u32, __builtin_convertvector(v, bf2)); }
DEVI bf2 asbf2(u32 v) { return __builtin_bit_cast(bf2, v); }
DEVI float rbf(float a) { return bl(pk2(a, a)); }
DEVI f2 cmul(f2 x, f2 y, f2 yr) { return __builtin_elementwise_fma(x.yy, yr, x.xx * y); }
DEVI f2 rot(f2 y) { return (f2){-y.y, y.x}; }
DEVI f2 cexp(float a2, float rev, float l) {
  float e = __builtin_amdgcn_exp2f(a2 * l), p = __builtin_amdgcn_fractf(rev * l);
  return (f2){e * __builtin_amdgcn_cosf(p), e * __builtin_amdgcn_sinf(p)};
}
DEVI float sigm(float v) { return __builtin_amdgcn_rcpf(1.f + __builtin_amdgcn_exp2f(v * -1.44269504f)); }
template <class T> DEVI T lds_ld(u32 a) { return *(const LAS T*)(uintptr_t)a; }
template <class T> DEVI void lds_st(u32 a, T v) { *(LAS T*)(uintptr_t)a = v; }
template <class T> DEVI T lds_ldv(u32 a) { return *(const volatile LAS T*)(uintptr_t)a; }
template <class T> DEVI void lds_stv(u32 a, T v) { *(volatile LAS T*)(uintptr_t)a = v; }
DEVI bf4 trd(u32 a) { return __builtin_amdgcn_ds_read_tr16_b64_v4bf16((LAS bf4*)(uintptr_t)a); }
DEVI bf8 cat(bf4 a, bf4 b) { return __builtin_shufflevector(a, b, 0, 1, 2, 3, 4, 5, 6, 7); }
DEVI f4 mma(bf8 a, bf8 b, f4 c) { return __builtin_amdgcn_mfma_f32_16x16x32_bf16(a, b, c, 0, 0, 0); }
DEVI u32 rl(u32 v, int l) { return __builtin_amdgcn_readlane(v, l); }
#define DPPF(v, ctl, rm) __uint_as_float(__builtin_amdgcn_update_dpp(0u, __float_as_uint(v), ctl, rm, 0xf, true))
#define DPPU(v, ctl, rm) __builtin_amdgcn_update_dpp(0u, v, ctl, rm, 0xf, true)
DEVI u32 umax(u32 a, u32 b) { return a > b ? a : b; }
DEVI u32 umin(u32 a, u32 b) { return a < b ? a : b; }
DEVI u32 wave_umax(u32 v) {
  v = umax(v, DPPU(v, 0x128, 0xf)); v = umax(v, DPPU(v, 0x124, 0xf)); v = umax(v, DPPU(v, 0x122, 0xf)); v = umax(v, DPPU(v, 0x121, 0xf));
  v = umax(v, DPPU(v, 0x142, 0xa)); v = umax(v, DPPU(v, 0x143, 0xc));
  return rl(v, 63);
}
#define MEMF() asm volatile("" ::: "memory")

#define GLD4_SC1(d, vo, base, imm) asm volatile("global_load_dwordx4 %0, %1, %2 offset:%3 sc1" : "=v"(d) : "v"(vo), "s"(base), "i"(imm) : "memory")
#define GLD1_SC1(d, vo, base, imm) asm volatile("global_load_dword %0, %1, %2 offset:%3 sc1" : "=v"(d) : "v"(vo), "s"(base), "i"(imm) : "memory")
#define GLD2_NT(d, vo, base, imm) asm volatile("global_load_dwordx2 %0, %1, %2 offset:%3 nt" : "=v"(d) : "v"(vo), "s"(base), "i"(imm) : "memory")
#define GLD1_NT(d, vo, base) asm volatile("global_load_dword %0, %1, %2 nt" : "=v"(d) : "v"(vo), "s"(base) : "memory")
#define GLDH(d, vo, base) asm volatile("global_load_ushort %0, %1, %2" : "=v"(d) : "v"(vo), "s"(base) : "memory")
#define GLD4(d, vo, base, imm) asm volatile("global_load_dwordx4 %0, %1, %2 offset:%3" : "=v"(d) : "v"(vo), "s"(base), "i"(imm) : "memory")
DEVI void dma16(u32 vo, const void* base, u32 ldsdst) {
  u32 keep;
  asm volatile("s_mov_b32 %0, m0\n\ts_mov_b32 m0, %3\n\ts_nop 0\n\tglobal_load_lds_dwordx4 %1, %2\n\ts_mov_b32 m0, %0" : "=&s"(keep) : "v"(vo), "s"(base), "s"(ldsdst) : "memory");
}
DEVI void dma16x4(u32 v0, u32 v1, u32 v2, u32 v3, const void* base, u32 ldsdst) {
  u32 keep;
  asm volatile("s_mov_b32 %0, m0\n\ts_mov_b32 m0, %6\n\ts_nop 0\n\tglobal_load_lds_dwordx4 %1, %5\n\tglobal_load_lds_dwordx4 %2, %5 offset:1024\n\t"
               "global_load_lds_dwordx4 %3, %5 offset:2048\n\tglobal_load_lds_dwordx4 %4, %5 offset:3072\n\ts_mov_b32 m0, %0"
               : "=&s"(keep) : "v"(v0), "v"(v1), "v"(v2), "v"(v3), "s"(base), "s"(ldsdst) : "memory");
}
DEVI void dma4nt(u32 vo, const void* base, u32 ldsdst) {
  u32 keep;
  asm volatile("s_mov_b32 %0, m0\n\ts_mov_b32 m0, %3\n\ts_nop 0\n\tglobal_load_lds_dword %1, %2 nt\n\ts_mov_b32 m0, %0" : "=&s"(keep) : "v"(vo), "s"(base), "s"(ldsdst) : "memory");
}
DEVI void dma4(u32 vo, const void* base, u32 ldsdst) {
  u32 keep;
  asm volatile("s_mov_b32 %0, m0\n\ts_mov_b32 m0, %3\n\ts_nop 0\n\tglobal_load_lds_dword %1, %2\n\ts_mov_b32 m0, %0" : "=&s"(keep) : "v"(vo), "s"(base), "s"(ldsdst) : "memory");
}

DEVI bf8 band(int lane, int co, int hw) {
  int lo = (lane & 15) + co - hw - 8 * (lane >> 4);
  u32 m = (u32)(((u64)((1u << (2 * hw + 1)) - 1) << (lo + 24)) >> 24);
  u4v r;
#pragma unroll
  for (int i = 0; i < 4; i++) r[i] = ((m >> (2 * i)) & 1u) * 0x3F80u + ((m >> (2 * i + 1)) & 1u) * 0x3F800000u;
  return __builtin_bit_cast(bf8, r);
}

struct Sched {
  int n, nl, nd;
  int kind[128], p0[128], p1[128], ld[128], tile[128];
  int loff[96];
  int dsrc[40];
  int last[4];
  int lcum[129], ccum[129], yat[4];
};
constexpr int PFD = 8;
constexpr int lswap(int k, int n) { return (k ^ 1) < n ? (k ^ 1) : k; }
constexpr int tile_of(int hf, int i) { return hf == 0 ? (i == 0 ? 0 : i == 1 ? 3 : i == 2 ? 4 : 7) : (i == 0 ? 1 : i == 1 ? 2 : i == 2 ? 5 : 6); }
struct Sched2 { Sched s; int dmap[32]; };
constexpr Sched2 make_sched(int hf) {
  Sched2 r{}; Sched& s = r.s;
  for (int i = 0; i < 4; i++) {
    const int q = tile_of(hf, i), pq = i ? tile_of(hf, i - 1) : -1;
    for (int e = 0; e < 6; e++) {
      const bool n2 = (e <= q - 2) && (e >= q - 4) && !((e <= pq - 2) && (e >= pq - 4));
      const bool n3 = (e <= q - 4) && !(e <= pq - 4);
      if (!(n2 || n3)) continue;
      const int l24 = s.nl++, l16 = s.nl++;
      s.loff[l24] = 64 * (8 * e + 7) - 16; s.loff[l16] = 64 * (8 * e + 7);
      const int bsel[6] = {0, 0, 1, 2, 3, 4}, lsel[6] = {l24, l16, l16, l16, l24, l16};
      const int dm[6] = {4 * e, 4 * e + 1, 4 * e + 2, 4 * e + 3, 24 + 2 * e, 24 + 2 * e + 1};
      for (int k = 0; k < 6; k++) {
        if (k < 4 ? !n2 : !n3) continue;
        s.kind[s.n] = 1; s.p0[s.n] = bsel[k]; s.p1[s.n] = s.nd; s.ld[s.n] = lsel[k]; s.tile[s.n] = i;
        r.dmap[dm[k]] = s.nd; s.dsrc[s.nd++] = s.n; s.n++;
      }
    }
    for (int m = 0; m < 17; m++) {
      const int pp = 8 * q - m;
      if (pp < -7) continue;
      s.loff[s.nl] = 64 * (pp + 7);
      s.kind[s.n] = 0; s.p0[s.n] = m; s.ld[s.n] = s.nl++; s.tile[s.n] = i; s.n++;
    }
    for (int a = 2; a <= 4; a++)
      for (int h = 0; h < 2; h++)
        if (q - a >= 0) { s.kind[s.n] = 2; s.p0[s.n] = 2 * (q - a) + h; s.p1[s.n] = 2 * (a - 2) + h; s.ld[s.n] = -1; s.tile[s.n] = i; s.n++; }
    for (int a = 4; a <= 7; a++)
      if (q - a >= 0) { s.kind[s.n] = 2; s.p0[s.n] = 12 + (q - a); s.p1[s.n] = 6 + (a - 4); s.ld[s.n] = -1; s.tile[s.n] = i; s.n++; }
    s.last[i] = s.n - 1;
    s.yat[i] = s.n + 1;
  }
  int nl = PFD, nc = 0, mx = 0;
  s.lcum[0] = nl; s.ccum[0] = 0;
  for (int i = 0; i < s.n; i++) {
    if (s.ld[i] > mx) mx = s.ld[i];
    int slots = 2;
    if (nl < s.nl && nl <= (mx | 1) + PFD - 1) { nl++; slots--; }
    while (slots > 0 && nc < 2 * s.nd && s.dsrc[nc / 2] <= i - 2) { nc++; slots--; }
    if (slots > 0 && nl < s.nl && nl <= (mx | 1) + PFD - 1) { nl++; slots--; }
    s.lcum[i + 1] = nl; s.ccum[i + 1] = nc;
  }
  return r;
}
template <int HF> struct SchedOf { static constexpr Sched2 v = make_sched(HF); };

struct ConvRegs { bf8 An[17], T[10], bands[5]; };
template <int HF, int I>
DEVI void conv_step(u32 ub, u32 yaddr, u32 faddr, const ConvRegs& R, bf8 (&fr)[96], f4 (&D)[40], u32 (&P)[80], f4 (&acc)[4]) {
  constexpr const Sched2& S2 = SchedOf<HF>::v; constexpr const Sched& S = S2.s;
  if constexpr (I < S.n) {
    const f4 z = {0.f, 0.f, 0.f, 0.f};
    constexpr int t = S.tile[I], p0 = S.p0[I], p1 = S.p1[I], ld = S.ld[I];
    if constexpr (S.kind[I] == 0) acc[t] = mma(R.An[p0], fr[ld], p0 == 0 ? z : acc[t]);
    else if constexpr (S.kind[I] == 1) D[p1] = mma(fr[ld], R.bands[p0], z);
    else {
      constexpr int d0 = S2.dmap[2 * S.p0[I]], d1 = S2.dmap[2 * S.p0[I] + 1];
      static_assert(S.ccum[I] >= 2 * d0 + 2 && S.ccum[I] >= 2 * d1 + 2, "window not converted in time");
      acc[t] = mma(__builtin_bit_cast(bf8, ((u4v){P[2 * d0], P[2 * d0 + 1], P[2 * d1], P[2 * d1 + 1]})), R.T[p1], acc[t]);
    }
    __builtin_amdgcn_sched_barrier(0);
    constexpr int l0 = S.lcum[I], l1 = S.lcum[I + 1], c0 = S.ccum[I], c1 = S.ccum[I + 1];
    if constexpr (l0 < l1) { constexpr int k = lswap(l0, S.nl), o = S.loff[k]; fr[k] = lds_ld<bf8>(ub + o); }
    if constexpr (l0 + 1 < l1) { constexpr int k = lswap(l0 + 1, S.nl), o = S.loff[k]; fr[k] = lds_ld<bf8>(ub + o); }
    if constexpr (c0 < c1) P[c0] = pk2(D[c0 / 2][2 * (c0 & 1)], D[c0 / 2][2 * (c0 & 1) + 1]);
    if constexpr (c0 + 1 < c1) P[c0 + 1] = pk2(D[(c0 + 1) / 2][2 * ((c0 + 1) & 1)], D[(c0 + 1) / 2][2 * ((c0 + 1) & 1) + 1]);
    constexpr int yi = S.yat[0] == I ? 0 : S.yat[1] == I ? 1 : S.yat[2] == I ? 2 : I == S.n - 1 ? 3 : -1;
    if constexpr (yi >= 0) {
      const f4 y = acc[yi] + 0.0f;
      asm volatile("ds_write_b128 %0, %1 offset:%2\n\tds_write_b32 %3, %4 offset:%5" :: "v"(yaddr), "v"(y), "i"(yi * 1024), "v"(faddr), "v"(yaddr), "i"(yi * 16));
    }
    __builtin_amdgcn_sched_barrier(0);
    conv_step<HF, I + 1>(ub, yaddr, faddr, R, fr, D, P, acc);
  }
}
template <int HF, int L>
DEVI void conv_pre(u32 ub, bf8 (&fr)[96]) {
  if constexpr (L < PFD) { constexpr int k = lswap(L, 99), o = SchedOf<HF>::v.s.loff[k]; fr[k] = lds_ld<bf8>(ub + o); conv_pre<HF, L + 1>(ub, fr); }
}
template <int HF>
DEVI void conv(u32 ub, u32 yaddr, u32 faddr, const ConvRegs& R) {
  bf8 fr[96]; f4 D[40]; u32 P[80]; f4 acc[4];
  conv_pre<HF, 0>(ub, fr);
  __builtin_amdgcn_sched_barrier(0);
  conv_step<HF, 0>(ub, yaddr, faddr, R, fr, D, P, acc);
}

__global__ void __launch_bounds__(512, 2)
s4_moe_kernel(const u16* x_, const u16* Are_, const u16* Aim_, const u16* Bre_, const u16* Bim_, const u16* Cre_,
              const u16* Cim_, const u16* ldt_, const u16* dsk_, const u16* wr_, const u16* wg_, const u16* wu_,
              const u16* wd_, u16* out_, char* ws_, u64 pad_) {
  extern __shared__ __attribute__((aligned(16))) char smem[];
  const int lane = threadIdx.x & 63;
  const int wid = __builtin_amdgcn_readfirstlane(threadIdx.x >> 6);
  const int w = blockIdx.x, xc = w & 7, mm = w >> 3;
  const int cg = xc * 16 + (mm >> 1), hf = mm & 1;
  const bool prod = mm < 14;
  STAMP_DECL;
  STAMP(0);
  typedef u32 u16v __attribute__((ext_vector_type(16)));
  u16v ka, kb_;
  asm volatile("s_load_dwordx16 %0, %2, 0x0\n\ts_load_dwordx16 %1, %2, 0x40\n\ts_waitcnt lgkmcnt(0)" : "=&s"(ka), "=&s"(kb_) : "s"(__builtin_amdgcn_kernarg_segment_ptr()) : "memory");
  STAMP(14);
#define KP(T, v, i) ((T)(((u64)v[2 * (i) + 1] << 32) | v[2 * (i)]))
  const u16* x = KP(const u16*, ka, 0); const u16* Are = KP(const u16*, ka, 1); const u16* Aim = KP(const u16*, ka, 2); const u16* Bre = KP(const u16*, ka, 3);
  const u16* Bim = KP(const u16*, ka, 4); const u16* Cre = KP(const u16*, ka, 5); const u16* Cim = KP(const u16*, ka, 6); const u16* ldt = KP(const u16*, ka, 7);
  const u16* dsk = KP(const u16*, kb_, 0); const u16* wr = KP(const u16*, kb_, 1); const u16* wg = KP(const u16*, kb_, 2); const u16* wu = KP(const u16*, kb_, 3);
  const u16* wd = KP(const u16*, kb_, 4); u16* out = KP(u16*, kb_, 5); char* ws = KP(char*, kb_, 6);

  if (wid < 4) {
    __builtin_amdgcn_s_setprio(2);
    const int d = 4 * cg + wid;
    u2v xt0, xt1, xt2, xt3, xt4, xt5, xt6, xt7;
    const int R = xc * 4 + (mm >> 3), S = mm & 7;
    if (wid < 2) {
      const u32 xp = (64 * R + 8 * (lane & 7)) * 1024 + 128 * S + 64 * wid + 8 * (lane >> 3), xq = xp + 4096;
      GLD2_NT(xt0, xp, x, 0); GLD2_NT(xt1, xp, x, 1024); GLD2_NT(xt2, xp, x, 2048); GLD2_NT(xt3, xp, x, 3072);
      GLD2_NT(xt4, xq, x, 0); GLD2_NT(xt5, xq, x, 1024); GLD2_NT(xt6, xq, x, 2048); GLD2_NT(xt7, xq, x, 3072);
    }
    const u32 po = d * 256 + lane * 4;
    u32 p_ar, p_ai, p_br, p_bi, p_cr, p_ci, p_dt;
    GLDH(p_dt, 2 * d, ldt);
    GLD1_NT(p_ar, po, Are); GLD1_NT(p_ai, po, Aim); GLD1_NT(p_br, po, Bre); GLD1_NT(p_bi, po, Bim); GLD1_NT(p_cr, po, Cre); GLD1_NT(p_ci, po, Cim);
    if (wid >= 2) {
      u64 pc; asm volatile("s_getpc_b64 %0" : "=s"(pc));
      const u32 ln = (wid - 2) * 64 + lane;
      dma4((ln < CODE_LINES ? ln : 0) * 128, (const void*)pc, M_DUM);
      dma4(0u, out + 4 * cg, M_DUM);
    }
    if (lane < 32) lds_st<u4v>(UB + wid * UCH + lane * 16, (u4v){0, 0, 0, 0});
    asm volatile("s_waitcnt lgkmcnt(0)\n\ts_barrier" ::: "memory");
    STAMP(6);
    ConvRegs CR;
    CR.bands[0] = band(lane, 8, 2); CR.bands[1] = band(lane, 12, 1); CR.bands[2] = band(lane, 4, 1); CR.bands[3] = band(lane, 8, 4); CR.bands[4] = band(lane, 8, 3);
    const u32 kb = wid * KREG;
    const int hh = lane >> 5, j = lane & 31, od = lane & 1, wh = lane >> 2;
    u32 a0 = kb + 32 + 16 * j + 8 * hh;
    u32 a1 = kb + (lane < 36 ? 544 + 16 * lane : lane < 38 ? 16 * (lane - 36) : O_Y);
    u32 tA = kb + (hh ? (od ? O_TB + 32 : O_TA + 30) : (od ? O_TF + 32 : O_TE + 30)) + (j >> 1) * 2;
    u32 tB = hh ? kb + (od ? O_TD : O_TC) + 32 + (j >> 1) * 2 : tA + 64;
    constexpr u64 SAT = (u64)(O_TA + 30) | ((u64)(O_TE + 30) << 12) | ((u64)(O_TA + 94) << 24) | ((u64)(O_TC + 96) << 36) | ((u64)(O_TE + 158) << 48);
    u32 sa = kb + (lane < 20 ? (lane & 3) * (CS + 2) + (u32)((SAT >> (12 * (wh < 5 ? wh : 0))) & 0xfff) : O_Y);
    u32 zA = kb + (lane < 52 ? O_TA + lane * 16 : 1952);
    u32 cA = kb + 16 * lane, pA = lane ? cA - 8 : cA, cA2 = kb + (lane < 6 ? 1024 + 16 * lane : 1952);
    u32 m1 = lane == 32 ? 0xffffffffu : 0u, m0 = hh ? 0u : 0xffffffffu;
    u32 bn = kb + CS * (wh & 3) + 56 - 16 * (lane >> 4) + 8 * (lane & 3), bt = kb + CS * (wh & 3) - 8 * (lane >> 4) + 8 * (lane & 3);
    u32 ta = kb + lane * 4, fa = kb + j * TROW + hh * 16;
    asm volatile("" : "+v"(CR.bands[0]), "+v"(CR.bands[1]), "+v"(CR.bands[2]), "+v"(CR.bands[3]), "+v"(CR.bands[4]), "+v"(a0), "+v"(a1), "+v"(tA), "+v"(tB),
                 "+v"(sa), "+v"(zA), "+v"(cA), "+v"(pA), "+v"(cA2), "+v"(m1), "+v"(m0), "+v"(bn), "+v"(bt), "+v"(ta), "+v"(fa));
    STAMP(7);
    if (wid >= 2) {
      __builtin_amdgcn_s_sleep(SLP_W);
      const u32 row = mm * 16 + (lane >> 2);
      const u32 vo = row * 3584 + ((xc * 448) & ~127) + (lane & 3) * 128;
      dma4nt(vo, wid == 2 ? wg : wu, M_DUM);
    } else {
      u2v tg; u32 spins = 0;
      do { tg = lds_ldv<u2v>(M_TAG); } while (tg.y == 0 && ++spins < SPIN_CAP);
      STAMP(8);
      asm volatile("s_waitcnt vmcnt(7)" : "+v"(xt0), "+v"(xt1), "+v"(xt2), "+v"(xt3), "+v"(xt4), "+v"(xt5), "+v"(xt6), "+v"(xt7) :: "memory");
      STAMP(9);
      const u2v xt[8] = {xt0, xt1, xt2, xt3, xt4, xt5, xt6, xt7};
      u32 c = tg.x;
#pragma unroll
      for (int i = 0; i < 8; i++) c += xt[i].x + xt[i].y;
      const __amdgpu_buffer_rsrc_t rs = __builtin_amdgcn_make_buffer_rsrc(ws + W_XB + (R * 8 + S) * XBS, 0, XBS, 0x00020000);
      const u32 o = (8 * wid + (lane >> 3)) * 512 + (lane & 7) * 16;
#pragma unroll
      for (int i = 0; i < 4; i++)
        __builtin_amdgcn_raw_buffer_store_b128((u4v){xt[2 * i].x, xt[2 * i].y, xt[2 * i + 1].x, xt[2 * i + 1].y}, rs, o + i * 128, 0, 16);
      __builtin_amdgcn_raw_buffer_store_b32(c, rs, 8192 + wid * 256 + lane * 4, 0, 16);
    }
    asm volatile("s_cmp_lt_u32 %7, 2\n\ts_cbranch_scc1 1f\n\ts_waitcnt vmcnt(3)\n1:\n\ts_waitcnt vmcnt(5)"
                 : "+v"(p_ar), "+v"(p_ai), "+v"(p_br), "+v"(p_bi), "+v"(p_cr), "+v"(p_ci), "+v"(p_dt) : "s"(wid) : "memory", "scc");
    STAMP(1);

    const float dt = rbf(__builtin_amdgcn_exp2f(bl(p_dt) * 1.44269504f));
    f16v kd = {};
#pragma unroll 1
    for (int h = 0; h < 2; h++) {
      const u32 sh = 16 - 16 * h;
      const float a2 = dt * bh(p_ar << sh) * 1.44269504f, rv = dt * bh(p_ai << sh) * 0.159154943f;
      const f2 Bc = {dt * bh(p_br << sh), dt * bh(p_bi << sh)}, Cc = {bh(p_cr << sh), bh(p_ci << sh)};
      const f2 G = cmul(Cc, Bc, rot(Bc));
      const f2 w1 = cexp(a2, rv, 1.f), w256 = cexp(a2, rv, 256.f);
      f2 v8 = cexp(a2, -rv, 8.f);
      const f2 w1r = rot(w1), w256r = rot(w256);
      const f2 w2 = cmul(w1, w1, w1r), w2r = rot(w2), w4 = cmul(w2, w2, w2r), w4r = rot(w4);
      const f2 v16 = cmul(v8, v8, rot(v8)), v16r = rot(v16);
#define TW(r, v) lds_st<u32>(ta + (r) * TROW, pk2((v).x, (v).y))
      f2 fz = G, ge = v16, go = v8, fh = cmul(G, w256, w256r);
      TW(0, fz); TW(21, ((f2){1.f, 0.f})); TW(22, go); TW(23, ge); TW(8, fh);
      f2 t2 = cmul(fh, w2, w2r); TW(9, t2); t2 = cmul(t2, w2, w2r); TW(10, t2); t2 = cmul(t2, w2, w2r); TW(11, t2);
#pragma unroll
      for (int i = 1; i < 16; i++) {
        go = cmul(go, v16, v16r); TW(22 + 2 * i, go);
        if (i < 15) { ge = cmul(ge, v16, v16r); TW(23 + 2 * i, ge); }
        if (i < 8) { fz = cmul(fz, w1, w1r); TW(i, fz); }
        else {
          fh = cmul(fh, w256, w256r);
          if (i < 10) { TW(12 + (i - 8), fh); f2 t4 = cmul(fh, w4, w4r); TW(14 + (i - 8), t4); }
          else if (i < 15) TW(16 + (i - 10), fh);
        }
      }
      bf8 tf[16];
#pragma unroll
      for (int s = 0; s < 8; s++) { tf[2 * s] = lds_ld<bf8>(fa + 32 * s); tf[2 * s + 1] = lds_ld<bf8>(fa + 21 * TROW + 32 * s); }
      __builtin_amdgcn_sched_barrier(0);
#pragma unroll
      for (int s = 0; s < 8; s++) kd = __builtin_amdgcn_mfma_f32_32x32x16_bf16(tf[2 * s], tf[2 * s + 1], kd, 0, 0, 0);
    }
#pragma unroll
    for (int q = 0; q < 4; q++) lds_st<u4v>(zA + q * CS, (u4v){0, 0, 0, 0});
    STAMP(2);
    {
      lds_st<u2v>(a0, (u2v){pk2(kd[0], kd[1]), pk2(kd[2], kd[3])});
      const float en = DPPF(kd[4], 0x130, 0xf);
      const u32 e00 = pk2(kd[4], kd[4]);
      lds_st<u4v>(a1, (u4v){e00 & (m0 | m1), (pk2(kd[5], kd[6]) & m0) | (e00 & m1 & 0xffffu), pk2(kd[6], kd[6]) & m0, pk2(kd[7], en) & m0});
      const u4v c = lds_ld<u4v>(cA), c2 = lds_ld<u4v>(cA2);
      const u2v p = lds_ld<u2v>(pA), p2 = lds_ld<u2v>(cA2 - 8);
      const u32 v01 = hh ? pk2(kd[4], kd[5]) : pk2(kd[8], kd[9]), v23 = hh ? pk2(kd[6], kd[7]) : pk2(kd[10], kd[11]);
#pragma unroll
      for (int q = 0; q < 4; q++) {
        lds_st<u16>(tA + q * (CS + 2), (u16)v01); lds_st<u16>(tA + q * (CS + 2) + 32, (u16)(v01 >> 16));
        lds_st<u16>(tB + q * (CS + 2), (u16)v23); lds_st<u16>(tB + q * (CS + 2) + 32, (u16)(v23 >> 16));
      }
      const u32 k8 = pk2(kd[8], kd[8]);
      const u32 al = rl(k8, 0), k2048 = rl(k8, 32);
      lds_st<u16>(sa, (u16)(lane < 8 ? 0u : lane < 16 ? al : k2048));
      lds_st<u32>(M_AL + wid * 4, al << 16);
#define SHIFTS(c, p, A) do { \
      const u32 s0 = __builtin_amdgcn_alignbit(p.y, p.x, 16), s1 = __builtin_amdgcn_alignbit(c.x, p.y, 16), s2 = __builtin_amdgcn_alignbit(c.y, c.x, 16), \
                s3 = __builtin_amdgcn_alignbit(c.z, c.y, 16), s4 = __builtin_amdgcn_alignbit(c.w, c.z, 16); \
      lds_st<u4v>(A + CS, (u4v){s1, s2, s3, s4}); lds_st<u4v>(A + 2 * CS, (u4v){p.y, c.x, c.y, c.z}); lds_st<u4v>(A + 3 * CS, (u4v){s0, s1, s2, s3}); } while (0)
      SHIFTS(c, p, cA);
      SHIFTS(c2, p2, cA2);
    }
    MEMF();
#pragma unroll
    for (int a = 2; a <= 4; a++) {
      CR.T[2 * (a - 2)] = cat(trd(bt + O_TA + 32 * a - 32), trd(bt + O_TB + 32 * a - 32));
      CR.T[2 * (a - 2) + 1] = cat(trd(bt + O_TC + 32 * a - 32), trd(bt + O_TD + 32 * a - 32));
    }
#pragma unroll
    for (int a = 4; a <= 7; a++) CR.T[6 + a - 4] = cat(trd(bt + O_TE + 32 * a - 96), trd(bt + O_TF + 32 * a - 96));
#pragma unroll
    for (int m = 0; m < 17; m++) CR.An[m] = cat(trd(bn + 64 * m + 8), trd(bn + 64 * m));
    u32 ub = UB + wid * UCH + 32 + 32 * (lane & 15) + 16 * (lane >> 4);
    u32 ya = kb + O_Y + 64 * (lane & 15) + 16 * (lane >> 4), fl = M_YF + wid * 4;
    asm volatile("" : "+v"(ub), "+v"(ya), "+v"(fl));
    STAMP(3);
    asm volatile("s_waitcnt lgkmcnt(0)\n\ts_barrier" ::: "memory");
    STAMP(4);
    if (hf == 0) conv<0>(ub, ya, fl, CR); else conv<1>(ub, ya, fl, CR);
    STAMP(5);
    asm volatile("s_waitcnt vmcnt(0) lgkmcnt(0)" ::: "memory");
    STAMP_FLUSH();
    return;
  }

  const int cw = wid - 4;
  const int sp = xc * 32 + mm;
  u32 ep = 0;
  if (cw == 0) ep = __hip_atomic_load((GAS u32*)(ws + W_EP), __ATOMIC_RELAXED, __HIP_MEMORY_SCOPE_AGENT);
  const u4v xr0 = __builtin_nontemporal_load((const GAS u4v*)(x + (size_t)(8 * sp + 2 * cw) * 512) + lane);
  const u4v xr1 = __builtin_nontemporal_load((const GAS u4v*)(x + (size_t)(8 * sp + 2 * cw + 1) * 512) + lane);
  const u4v wv = ((const GAS u4v*)wr)[lane];
  const u2v dk = *(const GAS u2v*)(dsk + 4 * cg);
  if (cw == 3) lds_st<u32>(MB + lane * 4, 0u);
  asm volatile("s_waitcnt lgkmcnt(0)\n\ts_barrier" ::: "memory");
  u32 tag;
  if (cw == 0) {
    tag = __builtin_amdgcn_readfirstlane(ep) + 1u;
    if (lane == 0) lds_stv<u2v>(M_TAG, (u2v){tag, 1u});
  }
  {
    const bf2 ones = __builtin_bit_cast(bf2, 0x3F803F80u);
    float s0 = 0, q0 = 0, d0 = 0, s1 = 0, q1 = 0, d1 = 0, sw = 0;
#pragma unroll
    for (int i = 0; i < 4; i++) {
      const bf2 a = asbf2(xr0[i]), b = asbf2(xr1[i]), c = asbf2(wv[i]);
      s0 = __builtin_amdgcn_fdot2_f32_bf16(a, ones, s0, false); q0 = __builtin_amdgcn_fdot2_f32_bf16(a, a, q0, false); d0 = __builtin_amdgcn_fdot2_f32_bf16(a, c, d0, false);
      s1 = __builtin_amdgcn_fdot2_f32_bf16(b, ones, s1, false); q1 = __builtin_amdgcn_fdot2_f32_bf16(b, b, q1, false); d1 = __builtin_amdgcn_fdot2_f32_bf16(b, c, d1, false);
      sw = __builtin_amdgcn_fdot2_f32_bf16(c, ones, sw, false);
    }
    auto sw32 = [](float a, float b) { auto r = __builtin_amdgcn_permlane32_swap(__float_as_uint(a), __float_as_uint(b), false, false); return __uint_as_float(r[0]) + __uint_as_float(r[1]); };
    float s = sw32(s0, s1), q = sw32(q0, q1), dd = sw32(d0, d1); sw = sw32(sw, sw);
#define RED(v) v += DPPF(v, 0x128, 0xf);
    s += DPPF(s, 0x128, 0xf); q += DPPF(q, 0x128, 0xf); dd += DPPF(dd, 0x128, 0xf); sw += DPPF(sw, 0x128, 0xf);
    s += DPPF(s, 0x124, 0xf); q += DPPF(q, 0x124, 0xf); dd += DPPF(dd, 0x124, 0xf); sw += DPPF(sw, 0x124, 0xf);
    s += DPPF(s, 0x122, 0xf); q += DPPF(q, 0x122, 0xf); dd += DPPF(dd, 0x122, 0xf); sw += DPPF(sw, 0x122, 0xf);
    s += DPPF(s, 0x121, 0xf); q += DPPF(q, 0x121, 0xf); dd += DPPF(dd, 0x121, 0xf); sw += DPPF(sw, 0x121, 0xf);
    s += DPPF(s, 0x142, 0xa); q += DPPF(q, 0x142, 0xa); dd += DPPF(dd, 0x142, 0xa); sw += DPPF(sw, 0x142, 0xa);
    const float mean = s * (1.f / 512.f);
    const float var = fmaxf(q * (1.f / 512.f) - mean * mean, 0.f);
    const float mb = rbf(mean), sb = rbf(__builtin_amdgcn_sqrtf(rbf(rbf(var) + 1e-5f)));
    const float r = (dd - mb * sw) * __builtin_amdgcn_rcpf(sb);
    const u32 ms = pk2(mb, sb), rb = pk2(r, r) & 0xffffu;
    const u32 ms0 = rl(ms, 31), ms1 = rl(ms, 63), rr = rl(rb, 31) | (rl(rb, 63) << 16);
    if (cw != 0) {
      u2v tg; u32 spins = 0;
      do { tg = lds_ldv<u2v>(M_TAG); } while (tg.y == 0 && ++spins < SPIN_CAP);
      tag = __builtin_amdgcn_readfirstlane(tg.x);
    }
    const __amdgpu_buffer_rsrc_t rs = __builtin_amdgcn_make_buffer_rsrc(ws + W_ST, 0, 16384, 0x00020000);
    if (lane == 0) __builtin_amdgcn_raw_buffer_store_b128((u4v){ms0, ms1, rr, ms0 + ms1 + rr + tag + tag - 1u}, rs, cw * 4096 + sp * 16, 0, 16);
  }
  STAMP(1);
  const u32 chs = tag + tag - 1u;
  u4v su0, su1, su2, su3, xu0, xu1, xu2, xu3; u32 xk;
  const u32 so = W_ST + (64 * cw + lane) * 16;
  const u32 xb = W_XB + ((8 * cw + (lane >> 3)) * 8 + xc) * XBS;
  const u32 xo = xb + (mm >> 1) * 512 + (lane & 7) * 16, xko = xb + 8192 + (mm >> 1) * 32 + (lane & 7) * 4;
  const char* ws1 = ws + 4096; const char* ws2 = ws + 8192; const char* ws3 = ws + 12288;
#define LOAD_S() do { GLD4_SC1(su0, so, ws, 0); GLD4_SC1(su1, so, ws1, 0); GLD4_SC1(su2, so, ws2, 0); GLD4_SC1(su3, so, ws3, 0); } while (0)
#define LOAD_X() do { GLD4_SC1(xu0, xo, ws, 0); GLD4_SC1(xu1, xo, ws, 128); GLD4_SC1(xu2, xo, ws, 256); GLD4_SC1(xu3, xo, ws, 384); GLD1_SC1(xk, xko, ws, 0); } while (0)
#define OK_S() ((((su0.x + su0.y + su0.z + chs) ^ su0.w) | ((su1.x + su1.y + su1.z + chs) ^ su1.w) | ((su2.x + su2.y + su2.z + chs) ^ su2.w) | ((su3.x + su3.y + su3.z + chs) ^ su3.w)) == 0)
#define SUM4(v) (v.x + v.y + v.z + v.w)
#define OK_X() (SUM4(xu0) + SUM4(xu1) + SUM4(xu2) + SUM4(xu3) + tag == xk)
  __builtin_amdgcn_s_sleep(SLP_S);
  LOAD_S();
  __builtin_amdgcn_s_sleep(SLP_X);
  LOAD_X();
  const u32 fb = FB + cw * 12288;
  const int fp = xc * 14 + mm;
#define PULL() do { \
    const u32 go = (128 * cw + (lane >> 1)) * 3584 + fp * 32 + (lane & 1) * 16; \
    constexpr u32 GS = 32 * 3584 - 1024; \
    dma16x4(go, go + GS, go + 2 * GS, go + 3 * GS, wg, fb); \
    dma16x4(go, go + GS, go + 2 * GS, go + 3 * GS, wu, fb + 4096); \
    const u32 dof = (16 * fp + (lane >> 4)) * 1024 + 256 * cw + (lane & 15) * 16; \
    dma16x4(dof, dof + 3072, dof + 6144, dof + 9216, wd, fb + 8192); } while (0)
  asm volatile("s_waitcnt vmcnt(5)" : "+v"(su0), "+v"(su1), "+v"(su2), "+v"(su3) :: "memory");
  STAMP(2);
  for (u32 spins = 0; !__all(OK_S()) && spins < SPIN_CAP; spins++) {
    asm volatile("s_sleep 2\n\tglobal_load_dwordx4 %0, %4, %5 sc1\n\tglobal_load_dwordx4 %1, %4, %6 sc1\n\tglobal_load_dwordx4 %2, %4, %7 sc1\n\t"
                 "global_load_dwordx4 %3, %4, %8 sc1\n\ts_waitcnt vmcnt(0)"
                 : "=&v"(su0), "=&v"(su1), "=&v"(su2), "=&v"(su3) : "v"(so), "s"(ws), "s"(ws1), "s"(ws2), "s"(ws3) : "memory");
  }
  STAMP(3);
  u32 k1, k2;
  {
    const u32 ib = 0xffffu - (512 * cw + 8 * lane);
    auto key = [&](u32 b, int i) { u32 o = b ^ ((u32)((int)b >> 31) | 0x80000000u); return (o & 0xffff0000u) | (ib - i); };
    k1 = key(su0.z << 16, 0); k2 = 0;
    auto ins = [&](u32 k) { k2 = umax(k2, umin(k1, k)); k1 = umax(k1, k); };
    ins(key(su0.z & 0xffff0000u, 1)); ins(key(su1.z << 16, 2)); ins(key(su1.z & 0xffff0000u, 3)); ins(key(su2.z << 16, 4));
    ins(key(su2.z & 0xffff0000u, 5)); ins(key(su3.z << 16, 6)); ins(key(su3.z & 0xffff0000u, 7));
    const u32 K1 = wave_umax(k1), K2 = wave_umax(k1 == K1 ? k2 : k1);
    if (lane == 0) lds_stv<u2v>(M_TOP + cw * 8, (u2v){K1, K2});
    u4v ta, tb; u32 spins = 0;
    do { ta = lds_ldv<u4v>(M_TOP); tb = lds_ldv<u4v>(M_TOP + 16); } while ((ta.x == 0 || ta.z == 0 || tb.x == 0 || tb.z == 0) && ++spins < SPIN_CAP);
    const u32 a1 = umax(ta.x, ta.z), a2 = umax(umin(ta.x, ta.z), umax(ta.y, ta.w)), b1 = umax(tb.x, tb.z), b2 = umax(umin(tb.x, tb.z), umax(tb.y, tb.w));
    k1 = __builtin_amdgcn_readfirstlane(umax(a1, b1)); k2 = __builtin_amdgcn_readfirstlane(umax(umin(a1, b1), umax(a2, b2)));
  }
  const int t0 = (0xffffu - (k1 & 0xffffu)) & 2047, t1 = (0xffffu - (k2 & 0xffffu)) & 2047;
  u4v xs0, xs1, xs2, xs3;
  if (prod) {
    const u32 xso = ((lane & 1) ? t1 : t0) * 1024 + 256 * cw + 16 * (lane >> 4);
    GLD4(xs0, xso, x, 0); GLD4(xs1, xso, x, 64); GLD4(xs2, xso, x, 128); GLD4(xs3, xso, x, 192);
  }
  if (w == 0 && cw == 0 && lane == 0) __hip_atomic_store((GAS u32*)(ws + W_EP), tag, __ATOMIC_RELAXED, __HIP_MEMORY_SCOPE_AGENT);
  if ((t0 >> 3) == 64 * cw + lane) { lds_st<u4v>(M_SEL, (u4v){su0.x, su0.y, su1.x, su1.y}); lds_st<u4v>(M_SEL + 16, (u4v){su2.x, su2.y, su3.x, su3.y}); }
  if ((t1 >> 3) == 64 * cw + lane) { lds_st<u4v>(M_SEL + 32, (u4v){su0.x, su0.y, su1.x, su1.y}); lds_st<u4v>(M_SEL + 48, (u4v){su2.x, su2.y, su3.x, su3.y}); }
  float na[8], nb[8];
  {
    const u32 msv[8] = {su0.x, su0.y, su1.x, su1.y, su2.x, su2.y, su3.x, su3.y};
#pragma unroll
    for (int i = 0; i < 8; i++) { na[i] = __builtin_amdgcn_rcpf(bh(msv[i])); nb[i] = -bl(msv[i]) * na[i]; }
  }
  asm volatile("" : "+v"(na[0]), "+v"(na[1]), "+v"(na[2]), "+v"(na[3]), "+v"(na[4]), "+v"(na[5]), "+v"(na[6]), "+v"(na[7]),
               "+v"(nb[0]), "+v"(nb[1]), "+v"(nb[2]), "+v"(nb[3]), "+v"(nb[4]), "+v"(nb[5]), "+v"(nb[6]), "+v"(nb[7]));
  STAMP(4);
#define XW "4"
  asm volatile("s_cmp_lt_u32 %5, 111\n\ts_cbranch_scc1 1f\n\ts_waitcnt vmcnt(0)\n1:\n\ts_waitcnt vmcnt(" XW ")"
               : "+v"(xu0), "+v"(xu1), "+v"(xu2), "+v"(xu3), "+v"(xk) : "s"(w - 1) : "memory", "scc");
  for (u32 spins = 0; !__all(OK_X()) && spins < SPIN_CAP; spins++) {
    asm volatile("s_sleep 1\n\tglobal_load_dwordx4 %0, %5, %7 sc1\n\tglobal_load_dwordx4 %1, %5, %7 offset:128 sc1\n\tglobal_load_dwordx4 %2, %5, %7 offset:256 sc1\n\t"
                 "global_load_dwordx4 %3, %5, %7 offset:384 sc1\n\tglobal_load_dword %4, %6, %7 sc1\n\ts_waitcnt vmcnt(0)"
                 : "=&v"(xu0), "=&v"(xu1), "=&v"(xu2), "=&v"(xu3), "=&v"(xk) : "v"(xo), "v"(xko), "s"(ws) : "memory");
  }
  STAMP(5);
  u32 xa[8] = {xu0.x, xu0.z, xu1.x, xu1.z, xu2.x, xu2.z, xu3.x, xu3.z};
  u32 xcv[8] = {xu0.y, xu0.w, xu1.y, xu1.w, xu2.y, xu2.w, xu3.y, xu3.w};
  {
    u4v c0, c1, c2, c3;
#pragma unroll
    for (int i = 0; i < 4; i++) {
      const int r0 = 2 * i, r1 = 2 * i + 1;
      const f2 p0 = __builtin_elementwise_fma((f2){bl(xa[r0]), bh(xa[r0])}, (f2){na[r0], na[r0]}, (f2){nb[r0], nb[r0]});
      const f2 p1 = __builtin_elementwise_fma((f2){bl(xa[r1]), bh(xa[r1])}, (f2){na[r1], na[r1]}, (f2){nb[r1], nb[r1]});
      const f2 p2 = __builtin_elementwise_fma((f2){bl(xcv[r0]), bh(xcv[r0])}, (f2){na[r0], na[r0]}, (f2){nb[r0], nb[r0]});
      const f2 p3 = __builtin_elementwise_fma((f2){bl(xcv[r1]), bh(xcv[r1])}, (f2){na[r1], na[r1]}, (f2){nb[r1], nb[r1]});
      c0[i] = pk2(p0.x, p1.x); c1[i] = pk2(p0.y, p1.y); c2[i] = pk2(p2.x, p3.x); c3[i] = pk2(p2.y, p3.y);
    }
    const u32 ua = UB + 512 + (64 * cw + lane) * 16;
    lds_st<u4v>(ua, c0); lds_st<u4v>(ua + UCH, c1); lds_st<u4v>(ua + 2 * UCH, c2); lds_st<u4v>(ua + 3 * UCH, c3);
  }
  asm volatile("s_waitcnt lgkmcnt(0)\n\ts_barrier" ::: "memory");
  STAMP(6);

  if (prod) {
    PULL();
    const u32 msel = lds_ld<u32>(M_SEL + (lane & 1) * 32 + (((lane & 1) ? t1 : t0) & 7) * 4);
    const float a = __builtin_amdgcn_rcpf(bh(msel)), b = -bl(msel) * a;
    asm volatile("s_waitcnt vmcnt(0)" : "+v"(xs0), "+v"(xs1), "+v"(xs2), "+v"(xs3) :: "memory");
    STAMP(7);
    auto nrm = [&](u4v v) { u4v r;
#pragma unroll
      for (int i = 0; i < 4; i++) r[i] = pk2(fmaf(bl(v[i]), a, b), fmaf(bh(v[i]), a, b));
      return __builtin_bit_cast(bf8, r); };
    const bf8 xf[4] = {nrm(xs0), nrm(xs1), nrm(xs2), nrm(xs3)};
    const int g = lane >> 4, q = (lane >> 2) & 3, p = lane & 3;
    const u32 ga = fb + (8 * g + q) * 32 + 8 * p;
    f4 ag = {0, 0, 0, 0}, au = {0, 0, 0, 0};
#pragma unroll
    for (int s = 0; s < 4; s++) {
      ag = mma(cat(trd(ga + s * 1024), trd(ga + s * 1024 + 128)), xf[s], ag);
      au = mma(cat(trd(ga + 4096 + s * 1024), trd(ga + 4096 + s * 1024 + 128)), xf[s], au);
    }
    if ((lane & 15) < 2) {
      lds_st<f4>(M_PART + cw * 256 + (lane & 1) * 64 + g * 16, ag);
      lds_st<f4>(M_PART + cw * 256 + 128 + (lane & 1) * 64 + g * 16, au);
    }
    MEMF();
    if (lane == 0) lds_stv<u32>(M_FF + cw * 4, 1u);
    u4v ff; u32 spins = 0;
    do { ff = lds_ldv<u4v>(M_FF); } while ((ff.x & ff.y & ff.z & ff.w) == 0 && ++spins < SPIN_CAP);
    MEMF();
    f4 sg = {0, 0, 0, 0}, su = {0, 0, 0, 0};
#pragma unroll
    for (int v = 0; v < 4; v++) {
      sg += lds_ld<f4>(M_PART + v * 256 + (lane & 1) * 64 + g * 16);
      su += lds_ld<f4>(M_PART + v * 256 + 128 + (lane & 1) * 64 + g * 16);
    }
    float hv[4];
#pragma unroll
    for (int i = 0; i < 4; i++) { const float gg = rbf(sg[i]); hv[i] = rbf(gg * sigm(gg)) * rbf(su[i]); }
    const s4v hb = __builtin_bit_cast(s4v, ((u2v){pk2(hv[0], hv[1]), pk2(hv[2], hv[3])}));
    const u32 da = fb + 8192 + g * 1024 + q * 256 + 8 * p;
    const __amdgpu_buffer_rsrc_t rs = __builtin_amdgcn_make_buffer_rsrc(ws + W_FB + fp * 8192, 0, 8192, 0x00020000);
    const u32 fo = (32 * cw + g) * 64 + (lane & 1) * 32 + (lane & 2) * 8;
#pragma unroll
    for (int t = 0; t < 8; t++) {
      const f4 y = __builtin_amdgcn_mfma_f32_16x16x16bf16_1k(__builtin_bit_cast(s4v, trd(da + 32 * t)), hb, (f4){0, 0, 0, 0}, 0, 0, 0);
      const u4v o = {__float_as_uint((lane & 2) ? y[2] : y[0]), tag, __float_as_uint((lane & 2) ? y[3] : y[1]), tag};
      if ((lane & 12) == 0) __builtin_amdgcn_raw_buffer_store_b128(o, rs, fo + t * 256, 0, 16);
    }
    STAMP(8);
  }

  const int up = (cw & 1) ^ hf;
  const int tile = 2 * cw + up;
  const int tl = 8 * (lane & 31) + 4 * (lane >> 5), tr0 = 256 * tile + tl;
  float xv[4][4], uv[4][4], pre[4][4], gt[4][4];
#pragma unroll
  for (int r = 0; r < 4; r++) {
    auto s0 = __builtin_amdgcn_permlane32_swap(xa[r], xa[r + 4], false, false);
    auto s1 = __builtin_amdgcn_permlane32_swap(xcv[r], xcv[r + 4], false, false);
    const u32 e0 = up ? s0[1] : s0[0], e1 = up ? s1[1] : s1[0];
    xv[0][r] = bl(e0); xv[1][r] = bh(e0); xv[2][r] = bl(e1); xv[3][r] = bh(e1);
  }
  {
    const f4 alv = lds_ld<f4>(M_AL);
    const float dv[4] = {bl(dk.x), bh(dk.x), bl(dk.y), bh(dk.y)};
    const u32 ua = UB + 512 + tr0 * 2;
    const u32 ca = tile >= 4 ? ua - 2064 : UB;
#pragma unroll
    for (int c = 0; c < 4; c++) {
      const u2v a = lds_ld<u2v>(ua + c * UCH), p = lds_ld<u2v>(ca + c * UCH), n = lds_ld<u2v>(ca + c * UCH + 8);
      uv[c][0] = bl(a.x); uv[c][1] = bh(a.x); uv[c][2] = bl(a.y); uv[c][3] = bh(a.y);
      pre[c][0] = uv[c][0] * dv[c] - alv[c] * bh(p.y); pre[c][1] = uv[c][1] * dv[c] - alv[c] * bl(n.x);
      pre[c][2] = uv[c][2] * dv[c] - alv[c] * bh(n.x); pre[c][3] = uv[c][3] * dv[c] - alv[c] * bl(n.y);
#pragma unroll
      for (int r = 0; r < 4; r++) gt[c][r] = uv[c][r] * sigm(uv[c][r]);
    }
  }
  STAMP(9);
  {
    u4v yf; u32 spins = 0;
    do { __builtin_amdgcn_s_sleep(1); yf = lds_ldv<u4v>(M_YF + cw * 16); } while (umin(umin(yf.x, yf.y), umin(yf.z, yf.w)) == 0 && ++spins < SPIN_CAP);
  }
  MEMF();
  STAMP(10);
#pragma unroll
  for (int c = 0; c < 4; c++) {
    const f4 y = lds_ld<f4>(c * KREG + O_Y + cw * 1024 + tl * 4);
#pragma unroll
    for (int r = 0; r < 4; r++) pre[c][r] += y[r];
  }
  const bool late = (tr0 >> 2) == (t0 >> 2) || (tr0 >> 2) == (t1 >> 2);
  char* op = (char*)out + (size_t)tr0 * 1024 + cg * 8;
  if (!late) {
#pragma unroll
    for (int r = 0; r < 4; r++)
      *(GAS u2v*)(op + r * 1024) = (u2v){pk2(fmaf(pre[0][r], gt[0][r], xv[0][r]), fmaf(pre[1][r], gt[1][r], xv[1][r])),
                                         pk2(fmaf(pre[2][r], gt[2][r], xv[2][r]), fmaf(pre[3][r], gt[3][r], xv[3][r]))};
  }
  STAMP(11);
  if ((t0 >> 8) == tile || (t1 >> 8) == tile) {
#pragma unroll 1
    for (int k = 0; k < 2; k++) {
      const int tk = k ? t1 : t0; const u32 kk = k ? k2 : k1;
      if ((tk >> 8) != tile) continue;
      const u32 fo = W_FB + (lane >> 1) * 8192 + cg * 64 + k * 32 + (lane & 1) * 16;
      const u32 fo3 = lane < 32 ? fo + 96 * 8192 : fo;
      u4v g0, g1, g2, g3;
      for (u32 spins = 0; spins < SPIN_CAP; spins++) {
        asm volatile("global_load_dwordx4 %0, %4, %8 sc1\n\tglobal_load_dwordx4 %1, %5, %8 sc1\n\tglobal_load_dwordx4 %2, %6, %8 sc1\n\t"
                     "global_load_dwordx4 %3, %7, %8 sc1\n\ts_waitcnt vmcnt(0)"
                     : "=&v"(g0), "=&v"(g1), "=&v"(g2), "=&v"(g3) : "v"(fo), "v"(fo + 32 * 8192), "v"(fo + 64 * 8192), "v"(fo3), "s"(ws) : "memory");
        if (__all(((g0.y ^ tag) | (g0.w ^ tag) | (g1.y ^ tag) | (g1.w ^ tag) | (g2.y ^ tag) | (g2.w ^ tag) | (g3.y ^ tag) | (g3.w ^ tag)) == 0)) break;
        __builtin_amdgcn_s_sleep(1);
      }
      float v0 = __uint_as_float(g0.x) + __uint_as_float(g1.x) + __uint_as_float(g2.x) + (lane < 32 ? __uint_as_float(g3.x) : 0.f);
      float v1 = __uint_as_float(g0.z) + __uint_as_float(g1.z) + __uint_as_float(g2.z) + (lane < 32 ? __uint_as_float(g3.z) : 0.f);
      v0 += DPPF(v0, 0x128, 0xf); v1 += DPPF(v1, 0x128, 0xf); v0 += DPPF(v0, 0x124, 0xf); v1 += DPPF(v1, 0x124, 0xf);
      v0 += DPPF(v0, 0x122, 0xf); v1 += DPPF(v1, 0x122, 0xf);
      float yv[4];
      {
        const u32 a = __float_as_uint(v0), b = __float_as_uint(v1);
        yv[0] = (__uint_as_float(rl(a, 0)) + __uint_as_float(rl(a, 16))) + (__uint_as_float(rl(a, 32)) + __uint_as_float(rl(a, 48)));
        yv[1] = (__uint_as_float(rl(b, 0)) + __uint_as_float(rl(b, 16))) + (__uint_as_float(rl(b, 32)) + __uint_as_float(rl(b, 48)));
        yv[2] = (__uint_as_float(rl(a, 1)) + __uint_as_float(rl(a, 17))) + (__uint_as_float(rl(a, 33)) + __uint_as_float(rl(a, 49)));
        yv[3] = (__uint_as_float(rl(b, 1)) + __uint_as_float(rl(b, 17))) + (__uint_as_float(rl(b, 33)) + __uint_as_float(rl(b, 49)));
      }
      const u32 ob = kk & 0xffff0000u;
      const float rs = rbf(sigm(__uint_as_float((ob & 0x80000000u) ? (ob ^ 0x80000000u) : (~ob & 0xffff0000u))));
#pragma unroll
      for (int c = 0; c < 4; c++) {
        const float upd = rbf(rbf(yv[c]) * rs);
#pragma unroll
        for (int r = 0; r < 4; r++) {
          const float b = rbf(uv[c][r] + upd);
          gt[c][r] = (tr0 + r == tk) ? b * sigm(b) : gt[c][r];
        }
      }
    }
    if (late) {
#pragma unroll
      for (int r = 0; r < 4; r++)
        *(GAS u2v*)(op + r * 1024) = (u2v){pk2(fmaf(pre[0][r], gt[0][r], xv[0][r]), fmaf(pre[1][r], gt[1][r], xv[1][r])),
                                           pk2(fmaf(pre[2][r], gt[2][r], xv[2][r]), fmaf(pre[3][r], gt[3][r], xv[3][r]))};
    }
    STAMP(12);
  }
  asm volatile("s_waitcnt vmcnt(0)" ::: "memory");
  STAMP(13);
  STAMP_FLUSH();
}

__global__ void k_a(char* ws) { if (threadIdx.x == 0) *(u64*)(ws + W_STAMP) = __builtin_amdgcn_s_memrealtime(); }
__global__ void k_b(char* ws) { u64 t = __builtin_amdgcn_s_memrealtime(); u64 e = *(volatile u64*)(ws + W_STAMP); u64 g = t - e; if (g > 3000) g = 3000; while (__builtin_amdgcn_s_memrealtime() < t + 5 * g) {} }
extern "C" void kernel_launch(void* const* d_in, const int* in_sizes, int n_in, void* d_out, int out_size,
                              void* d_ws, size_t ws_size, hipStream_t stream) {
  (void)hipFuncSetAttribute((const void*)s4_moe_kernel, hipFuncAttributeMaxDynamicSharedMemorySize, LDS_BYTES);
  s4_moe_kernel<<<256, 512, LDS_BYTES, stream>>>(
      (const u16*)d_in[0], (const u16*)d_in[1], (const u16*)d_in[2], (const u16*)d_in[3], (const u16*)d_in[4],
      (const u16*)d_in[5], (const u16*)d_in[6], (const u16*)d_in[7], (const u16*)d_in[8], (const u16*)d_in[10],
      (const u16*)d_in[11], (const u16*)d_in[12], (const u16*)d_in[13], (u16*)d_out, (char*)d_ws, 0ull);
  k_b<<<1, 64, 0, stream>>>((char*)d_ws);
}
```

```cpp
#define SLP_F 28
#include <hip/hip_runtime.h>
#include <hip/hip_bf16.h>
#include <stdint.h>

typedef unsigned short u16;
typedef unsigned int u32;
typedef unsigned long long u64;
typedef float f2 __attribute__((ext_vector_type(2)));
typedef float f4 __attribute__((ext_vector_type(4)));
typedef float f16v __attribute__((ext_vector_type(16)));
typedef u32 u2v __attribute__((ext_vector_type(2)));
typedef u32 u4v __attribute__((ext_vector_type(4)));
typedef __bf16 bf2 __attribute__((ext_vector_type(2)));
typedef __bf16 bf4 __attribute__((ext_vector_type(4)));
typedef __bf16 bf8 __attribute__((ext_vector_type(8)));
typedef short s4v __attribute__((ext_vector_type(4)));
#define LAS __attribute__((address_space(3)))
#define GAS __attribute__((address_space(1)))
#define DEVI __device__ __forceinline__

#define SLP_W 4
#define S_AT 10
#define SLP_G "6"
#define SLP_A "2"
#define SLP_K "0"
#define SLP_X 4
#define SLP_P "2"
#define PULL_AT 2
#define X_AT 14
#define CODE_BYTES 16896
#define NO_TOUCH
#define WNT ""
#define W_AT0 1
#define W_AT1 5
#define W_AT2 10
#define G_EARLY 1
#define SLP_L "8"
#define EARLY_W 0
#define WARM 3
#define WARM_D 1
#define W_EARLY 0
#define PNT ""
#define XNT ""
#define SPIN_CAP (1u << 18)
#define SLP_Y 1
#define SLP_FP 6
#define PRIO_S 2
#define PRIO_KS "1"
#define PRIO_C 1
#define PRIO_K 0

constexpr int KREG = 14592;
constexpr int TROW = 272;
constexpr int CS = 2112;
constexpr int O_TA = 1120, O_TB = 1248, O_TC = 1376, O_TD = 1504, O_TE = 1632, O_TF = 1792;
constexpr int O_Y = 8448;
constexpr int CB = 4 * KREG;
constexpr int UB = 8 * KREG;
constexpr int UCH = 4608;
constexpr int MB = UB + 4 * UCH;
constexpr int M_TAG = MB;
constexpr int M_TOP = MB + 16;
constexpr int M_YF = MB + 64;
constexpr int M_TF = MB + 128;
constexpr int M_RF = MB + 144;
constexpr int M_UF = MB + 160;
constexpr int M_AF = MB + 176;
constexpr int M_PART = MB + 256;
constexpr int M_DUM = MB + 1280;
constexpr int M_FR = MB + 2560;
constexpr int M_LC = MB + 3584;
constexpr int LDS_BYTES = M_LC + 16384;
constexpr u32 SENT = 0xffffffffu;

constexpr u32 W_EP = 0;
constexpr u32 W_ST = 4096;
constexpr u32 W_XB = 65536;
constexpr u32 XBS = 9216;
constexpr u32 W_FB = 4u << 20;
constexpr u32 W_FH = 32768;
constexpr u32 W_STAMP = 8u << 20;

#define ZS(z, i) do {} while (0)
#define STAMP(i) do {} while (0)
#define STAMP_DECL do {} while (0)
#define STAMP_FLUSH() do {} while (0)

DEVI float bl(u32 v) { return __uint_as_float(v << 16); }
DEVI float bh(u32 v) { return __uint_as_float(v & 0xffff0000u); }
DEVI u32 pk2(float a, float b) { f2 v = {a, b}; return __builtin_bit_cast(u32, __builtin_convertvector(v, bf2)); }
DEVI bf2 asbf2(u32 v) { return __builtin_bit_cast(bf2, v); }
DEVI float rbf(float a) { return bl(pk2(a, a)); }
DEVI f2 cmul(f2 x, f2 y, f2 yr) { return __builtin_elementwise_fma(x.yy, yr, x.xx * y); }
DEVI f2 rot(f2 y) { return (f2){-y.y, y.x}; }
DEVI f2 cexp(float a2, float rev, float l) {
  float e = __builtin_amdgcn_exp2f(a2 * l), p = __builtin_amdgcn_fractf(rev * l);
  return (f2){e * __builtin_amdgcn_cosf(p), e * __builtin_amdgcn_sinf(p)};
}
DEVI float sigm(float v) { return __builtin_amdgcn_rcpf(1.f + __builtin_amdgcn_exp2f(v * -1.44269504f)); }
template <class T> DEVI T lds_ld(u32 a) { return *(const LAS T*)(uintptr_t)a; }
template <class T> DEVI void lds_st(u32 a, T v) { *(LAS T*)(uintptr_t)a = v; }
template <class T> DEVI T lds_ldv(u32 a) { return *(const volatile LAS T*)(uintptr_t)a; }
template <class T> DEVI void lds_stv(u32 a, T v) { *(volatile LAS T*)(uintptr_t)a = v; }
DEVI bf4 trd(u32 a) { return __builtin_amdgcn_ds_read_tr16_b64_v4bf16((LAS bf4*)(uintptr_t)a); }
DEVI bf8 cat(bf4 a, bf4 b) { return __builtin_shufflevector(a, b, 0, 1, 2, 3, 4, 5, 6, 7); }
DEVI f4 mma(bf8 a, bf8 b, f4 c) { return __builtin_amdgcn_mfma_f32_16x16x32_bf16(a, b, c, 0, 0, 0); }
DEVI u32 rl(u32 v, int l) { return __builtin_amdgcn_readlane(v, l); }
#define DPPF(v, ctl, rm) __uint_as_float(__builtin_amdgcn_update_dpp(0u, __float_as_uint(v), ctl, rm, 0xf, true))
#define DPPU(v, ctl, rm) __builtin_amdgcn_update_dpp(0u, v, ctl, rm, 0xf, true)
DEVI u32 umax(u32 a, u32 b) { return a > b ? a : b; }
DEVI u32 umin(u32 a, u32 b) { return a < b ? a : b; }
DEVI u32 wave_umax(u32 v) {
  v = umax(v, DPPU(v, 0x128, 0xf)); v = umax(v, DPPU(v, 0x124, 0xf)); v = umax(v, DPPU(v, 0x122, 0xf)); v = umax(v, DPPU(v, 0x121, 0xf));
  v = umax(v, DPPU(v, 0x142, 0xa)); v = umax(v, DPPU(v, 0x143, 0xc));
  return rl(v, 63);
}
#define MEMF() asm volatile("" ::: "memory")

DEVI void dma16x4(u32 v0, u32 v1, u32 v2, u32 v3, const void* base, u32 ldsdst) {
  u32 keep;
  asm volatile("s_mov_b32 %0, m0\n\ts_mov_b32 m0, %6\n\ts_nop 0\n\tglobal_load_lds_dwordx4 %1, %5\n\tglobal_load_lds_dwordx4 %2, %5 offset:1024\n\t"
               "global_load_lds_dwordx4 %3, %5 offset:2048\n\tglobal_load_lds_dwordx4 %4, %5 offset:3072\n\ts_mov_b32 m0, %0"
               : "=&s"(keep) : "v"(v0), "v"(v1), "v"(v2), "v"(v3), "s"(base), "s"(ldsdst) : "memory");
}
#define PIN(...) asm volatile("" : __VA_ARGS__)
#define V(x) "+v"(x)
DEVI u32 ssel(bool c, u32 a, u32 b) { asm("" : "+s"(a), "+s"(b)); return c ? a : b; }
DEVI u32 vsel(bool c, u32 a, u32 b) { asm("" : "+v"(a), "+v"(b)); return c ? a : b; }
DEVI float dot2(u32 a, u32 b, float c) { return __builtin_amdgcn_fdot2_f32_bf16(asbf2(a), asbf2(b), c, false); }
DEVI u32 umed3(u32 a, u32 b, u32 c) { return umax(umin(a, b), umin(umax(a, b), c)); }

struct LaneTab { u32 v[16][64][4]; };
constexpr u32 band_dw(int lane, int i, int co, int hw, u32 one) {
  u32 r = 0;
  for (int h = 0; h < 2; h++) { const int d = 8 * (lane >> 4) + 2 * i + h - (lane & 15) - co; if (d >= -hw && d <= hw) r |= one << (16 * h); }
  return r;
}
constexpr LaneTab make_tab() {
  LaneTab t{};
  for (int l = 0; l < 64; l++) {
    const int hh = l >> 5, j = l & 31, od = l & 1, g = l >> 4, q = (l >> 2) & 3, p = l & 3;
    const int co[6] = {8, 12, 4, 8, 8, 11}, hw[6] = {2, 1, 1, 4, 3, 0};
    for (int s = 0; s < 6; s++) for (int i = 0; i < 4; i++) t.v[s][l][i] = band_dw(l, i, co[s], hw[s], s == 5 ? 0xffffu : 0x3F80u);
    const int sat[5] = {O_TA + 30, O_TE + 30, O_TA + 94, O_TC + 96, O_TE + 158};
    const u32 tA = (hh ? (od ? O_TB + 32 : O_TA + 30) : (od ? O_TF + 32 : O_TE + 30)) + (j >> 1) * 2;
    t.v[6][l][0] = 32 + 16 * j + 8 * hh;
    t.v[6][l][1] = l < 36 ? 544 + 16 * l : l < 38 ? 16 * (l - 36) : O_Y;
    t.v[6][l][2] = tA;
    t.v[6][l][3] = hh ? (od ? O_TD : O_TC) + 32 + (j >> 1) * 2 : tA + 64;
    t.v[7][l][0] = l < 5 ? sat[l] : O_Y;
    t.v[7][l][1] = l < 52 ? O_TA + l * 16 : 1952;
    t.v[7][l][2] = l ? 16 * l - 8 : 0;
    t.v[7][l][3] = 1024 + 16 * (l < 58 ? l : 58);
    t.v[8][l][0] = CS * q + 56 - 16 * g + 8 * p;
    t.v[8][l][1] = CS * q + 24 - 8 * g + 8 * p;
    t.v[8][l][2] = O_Y + 64 * (l & 15) + 16 * g;
    t.v[8][l][3] = UB + 32 + 32 * (l & 15) + 16 * g;
    t.v[9][l][0] = l < 33 ? 0xffffffffu : 0u;
    t.v[9][l][1] = hh ? 0u : 0xffffffffu;
    t.v[9][l][2] = l == 32 ? 0xffffu : 0u;
    t.v[9][l][3] = 0;
    t.v[10][l][0] = W_XB + g * 8 * XBS + (l & 15) * 16;
    t.v[10][l][1] = W_XB + g * 8 * XBS + 8192 + (l & 7) * 8 + ((l >> 3) & 1) * 4;
    t.v[10][l][2] = UB + 512 + 8 * l;
    t.v[10][l][3] = (l >> 1) * 3584 + od * 16;
    for (int i = 0; i < 2; i++) t.v[11][l][i] = g * 1024 + ((((l & 15) >> 1) ^ (4 * i + g)) * 2 + od) * 16;
    t.v[11][l][2] = (8 * g + q) * 32 + 8 * p;
    t.v[11][l][3] = M_PART + od * 64 + g * 16;
    for (int i = 0; i < 8; i++) { const int r = (i - q) & 3; t.v[12 + (i >> 2)][l][i & 3] = 8192 + g * 1024 + r * 256 + (i ^ (4 * (g & 1) + r)) * 32 + 8 * p; }
    for (int i = 0; i < 4; i++) t.v[14][l][i] = q == i ? 0xffffffffu : 0u;
    t.v[15][l][0] = M_PART + od * 64 + g * 16 + q * 4;
    t.v[15][l][1] = (l & 2) * 256 + (4 * q + g) * 32 + od * 16;
    const int c = g;
    t.v[15][l][2] = c * KREG + O_Y;
    t.v[15][l][3] = UB + 512 + c * UCH;
  }
  return t;
}
static __device__ const LaneTab LT = make_tab();

struct Sched {
  int n, nl, nd;
  int kind[128], p0[128], p1[128], ld[128], tile[128];
  int loff[96];
  int dsrc[40];
  bool first[128]; int tord[10], tpos[10], ntl, luse[96];
  int lcum[129], ccum[129], tcum[129], yat[4];
};
#define PFD_ 6
constexpr int PFD = PFD_;
#define LAT 6
#define LMAX 12
#define CDIST 5
constexpr int lswap(int k, int n) { return (k ^ 1) < n ? (k ^ 1) : k; }
constexpr int tile_of(int hf, int i) { return hf == 0 ? (i < 2 ? i : i + 4) : i + 2; }
struct Sched2 {
  Sched s; int dmap[32]; bool used[4];
  constexpr int ldp(int p) { s.loff[s.nl] = 64 * (p + 7); return s.nl++; }
  constexpr int ld24(int e) { s.loff[s.nl] = 64 * (8 * e + 7) - 16; return s.nl++; }
  constexpr void op(int kind, int p0, int p1, int ld, int t) {
    s.kind[s.n] = kind; s.p0[s.n] = p0; s.p1[s.n] = p1; s.ld[s.n] = ld; s.tile[s.n] = t;
    if (kind != 1) { s.first[s.n] = !used[t]; used[t] = true; }
    s.n++;
  }
  constexpr void near(int t, int m, int ld) { op(0, m, 0, ld, t); }
  constexpr void win1(int band, int dm, int ld) { dmap[dm] = s.nd; s.dsrc[s.nd] = s.n; op(1, band, s.nd++, ld, 0); }
  constexpr void win2(int e, int l24, int l16) { win1(0, 4 * e, l24); win1(0, 4 * e + 1, l16); win1(1, 4 * e + 2, l16); win1(2, 4 * e + 3, l16); }
  constexpr void win3(int e, int l24, int l16) { win1(3, 24 + 2 * e, l24); win1(4, 24 + 2 * e + 1, l16); }
  constexpr void far2(int t, int q, int a) { op(2, 2 * (q - a), 2 * (a - 2), -1, t); op(2, 2 * (q - a) + 1, 2 * (a - 2) + 1, -1, t); }
  constexpr void far3(int t, int q, int a) { op(2, 12 + (q - a), 6 + (a - 4), -1, t); }
  constexpr void done(int t) { s.yat[t] = s.n + 3; }
};
constexpr Sched2 make_sched(int hf) {
  Sched2 r{}; Sched& s = r.s;
  if (hf == 0) {
    int l[16] = {};
    for (int p = 0; p >= -7; p--) { l[p + 7] = r.ldp(p); r.near(0, -p, l[p + 7]); }
    r.done(0);
    for (int p = -7; p <= 0; p++) r.near(1, 8 - p, l[p + 7]);
    { const int a = r.ld24(0); r.win3(0, a, l[7]); }
    int l8 = 0;
    for (int p = 1; p <= 8; p++) { l8 = r.ldp(p); r.near(1, 8 - p, l8); }
    r.done(1);
    { const int a = r.ld24(1); r.win3(1, a, l8); }
    for (int e = 2; e < 4; e++) { const int a = r.ld24(e), c = r.ldp(8 * e); r.win2(e, a, c); r.win3(e, a, c); r.near(e, 17, c); }
    for (int p = 32; p <= 48; p++) {
      int a = 0;
      if (p == 32 || p == 40) a = r.ld24(p / 8);
      const int c = r.ldp(p);
      if (p == 32 || p == 40) r.win2(p / 8, a, c);
      r.near(2, 48 - p, c);
      if (p >= 40) r.near(3, 56 - p, c);
      if (p == 35) { r.far3(2, 6, 6); r.far3(2, 6, 5); r.far3(2, 6, 4); }
      if (p == 37) r.far2(2, 6, 4);
      if (p == 39) r.far2(2, 6, 3);
    }
    r.far2(2, 6, 2);
    r.done(2);
    for (int p = 49; p <= 56; p++) {
      r.near(3, 56 - p, r.ldp(p));
      if (p == 50) { r.far3(3, 7, 7); r.far3(3, 7, 6); }
      if (p == 52) { r.far3(3, 7, 5); r.far3(3, 7, 4); }
      if (p == 54) r.far2(3, 7, 4);
      if (p == 55) r.far2(3, 7, 3);
    }
    r.far2(3, 7, 2);
    r.done(3);
  } else {
    for (int p = 0; p <= 40; p++) {
      int a = 0;
      const bool w = (p & 7) == 0 && p <= 24;
      if (w) a = r.ld24(p / 8);
      const int c = r.ldp(p);
      for (int t = 0; t < 4; t++) { const int m = 8 * (t + 2) - p; if (m >= 0 && m <= 16) r.near(t, m, c); }
      if (w) r.win2(p / 8, a, c);
      if (w && p <= 8) { r.win3(p / 8, a, c); r.near(2 + p / 8, 17, c); }
      if (p == 16) { r.far2(0, 2, 2); r.done(0); }
      if (p == 20) r.far2(1, 3, 3);
      if (p == 24) { r.far2(1, 3, 2); r.done(1); }
      if (p == 27) { r.far2(2, 4, 4); r.far3(2, 4, 4); }
      if (p == 30) r.far2(2, 4, 3);
      if (p == 32) { r.far2(2, 4, 2); r.done(2); }
      if (p == 34) { r.far3(3, 5, 5); r.far3(3, 5, 4); }
      if (p == 36) r.far2(3, 5, 4);
      if (p == 38) r.far2(3, 5, 3);
    }
    r.far2(3, 5, 2);
    r.done(3);
  }
  for (int i = 0; i < s.n; i++)
    if (s.kind[i] == 2) {
      bool seen = false;
      for (int k = 0; k < s.ntl; k++) seen = seen || s.tord[k] == s.p1[i];
      if (!seen) { s.tpos[s.p1[i]] = s.ntl; s.tord[s.ntl++] = s.p1[i]; }
    }
  int fuse[96] = {}, cdl[80] = {}, tdl[20] = {};
  for (int l = 0; l < s.nl; l++) fuse[l] = 999;
  for (int c = 0; c < 80; c++) cdl[c] = 999;
  for (int k = 0; k < 20; k++) tdl[k] = 999;
  for (int i = s.n - 1; i >= 0; i--) {
    if (s.ld[i] >= 0) { fuse[s.ld[i]] = i; if (s.luse[s.ld[i]] == 0) s.luse[s.ld[i]] = i + 1; }
    if (s.kind[i] == 2) {
      const int d0 = r.dmap[2 * s.p0[i]], d1 = r.dmap[2 * s.p0[i] + 1];
      cdl[2 * d0] = cdl[2 * d0 + 1] = cdl[2 * d1] = cdl[2 * d1 + 1] = i - 3;
      tdl[2 * s.tpos[s.p1[i]]] = tdl[2 * s.tpos[s.p1[i]] + 1] = i - LAT;
    }
  }
  for (int c = 2 * s.nd - 2; c >= 0; c--) if (cdl[c] > cdl[c + 1]) cdl[c] = cdl[c + 1];
  int nl = PFD, nc = 0, nt = 0, waited = -1;
  s.lcum[0] = nl; s.ccum[0] = 0; s.tcum[0] = 0;
  for (int i = 0; i < s.n; i++) {
    int cap = 2;
    if (i + 1 < s.n && s.ld[i + 1] > waited) { cap = 1; waited = s.ld[i + 1] | 1; }
    if (i == s.yat[0] || i == s.yat[1] || i == s.yat[2]) cap = 0;
    for (int k = 0; k < 3; k++) {
      const int lu = nl < s.nl ? (fuse[nl & ~1] < fuse[lswap(nl, s.nl)] ? fuse[nl & ~1] : fuse[lswap(nl, s.nl)]) : 999;
      const int dl = nl < s.nl && lu - i <= LMAX ? lu - LAT : 999;
      const int dc = nc < 2 * s.nd && s.dsrc[nc / 2] <= i - CDIST ? cdl[nc] - (2 * s.nd - nc > 8 ? 8 : 0) : 999;
      const int dt = nt < 2 * s.ntl ? tdl[nt] : 999;
      const int m = dl < dc ? (dl < dt ? dl : dt) : (dc < dt ? dc : dt);
      if (m == 999 || (k >= cap && m > i) || (k >= 1 && m > i + 6)) break;
      if (m == dl) nl++; else if (m == dc) nc++; else nt++;
    }
    s.lcum[i + 1] = nl; s.ccum[i + 1] = nc; s.tcum[i + 1] = nt;
  }
  return r;
}
template <int HF> struct SchedOf { static constexpr Sched2 v = make_sched(HF); };

#define PASS_B "buffer_load_dwordx4 v[160:163], %[fo], %[rf], 0 offen sc1\n\t" "buffer_load_dwordx4 v[164:167], %[fo], %[rf], %[s1] offen sc1\n\t" "buffer_load_dwordx4 v[168:171], %[fo], %[rf], %[s2] offen sc1\n\t" "buffer_load_dwordx4 v[172:175], %[f3], %[rf], %[s3] offen sc1\n\t"
#define CHK_B "v_bitop3_b32 %[a], v161, v163, %[k] bitop3:0x7e\n\t" "v_bitop3_b32 %[c], v165, v167, %[k] bitop3:0x7e\n\tv_or_b32 %[a], %[a], %[c]\n\t" "v_bitop3_b32 %[c], v169, v171, %[k] bitop3:0x7e\n\tv_or_b32 %[a], %[a], %[c]\n\t" "v_bitop3_b32 %[c], v173, v175, %[k] bitop3:0x7e\n\tv_or_b32 %[a], %[a], %[c]\n\t" "v_cmp_ne_u32 vcc, 0, %[a]\n\t"
struct Hook { u4v rf; u32 fo, af, want, any; };
#define G_AT 10
struct ConvRegs { bf8 An[18], bands[5]; };
constexpr int toff(int k) {
  return k < 12 ? ((k & 2) ? ((k & 1) ? O_TD : O_TC) : ((k & 1) ? O_TB : O_TA)) + 32 * (2 + (k >> 2)) - 56 : ((k & 1) ? O_TF : O_TE) + 32 * (4 + ((k - 12) >> 1)) - 120;
}
template <bool FIRST> DEVI void macc(f4& c, bf8 a, bf8 b) {
  if constexpr (FIRST) asm volatile("v_mfma_f32_16x16x32_bf16 %0, %1, %2, 0" : "=&v"(c) : "v"(a), "v"(b));
  else asm volatile("v_mfma_f32_16x16x32_bf16 %0, %1, %2, %0" : "+v"(c) : "v"(a), "v"(b));
}
template <int HF, int I>
DEVI void conv_step(u32 ub, u32 bt, u32 yaddr, u32 faddr, const ConvRegs& R, bf8 (&fr)[96], bf4 (&T)[20], f4 (&D)[40], u32 (&P)[80], f4 (&acc)[4]) {
  constexpr const Sched2& S2 = SchedOf<HF>::v; constexpr const Sched& S = S2.s;
  if constexpr (I < S.n) {
    const f4 z = {0.f, 0.f, 0.f, 0.f};
    constexpr int t = S.tile[I], p0 = S.p0[I], p1 = S.p1[I], ld = S.ld[I];
    if constexpr (S.kind[I] == 0) macc<S.first[I]>(acc[t], R.An[p0], fr[ld]);
    else if constexpr (S.kind[I] == 1) D[p1] = mma(fr[ld], R.bands[p0], z);
    else {
      constexpr int d0 = S2.dmap[2 * S.p0[I]], d1 = S2.dmap[2 * S.p0[I] + 1];
      static_assert(S.ccum[I - 2] >= 2 * d0 + 2 && S.ccum[I - 2] >= 2 * d1 + 2, "window not converted in time");
      static_assert(S.tcum[I] >= 2 * S.tpos[p1] + 2, "far operand not loaded in time");
      macc<false>(acc[t], __builtin_bit_cast(bf8, ((u4v){P[2 * d0], P[2 * d0 + 1], P[2 * d1], P[2 * d1 + 1]})), cat(T[2 * p1], T[2 * p1 + 1]));
    }
    __builtin_amdgcn_sched_barrier(0);
    constexpr int l0 = S.lcum[I], l1 = S.lcum[I + 1], c0 = S.ccum[I], c1 = S.ccum[I + 1];
    if constexpr (l0 < l1) { constexpr int k = lswap(l0, S.nl), o = S.loff[k]; fr[k] = lds_ld<bf8>(ub + o); }
    if constexpr (l0 + 1 < l1) { constexpr int k = lswap(l0 + 1, S.nl), o = S.loff[k]; fr[k] = lds_ld<bf8>(ub + o); }
    if constexpr (l0 + 2 < l1) { constexpr int k = lswap(l0 + 2, S.nl), o = S.loff[k]; fr[k] = lds_ld<bf8>(ub + o); }
    if constexpr (I >= 2) { constexpr int k = S.ld[I - 2]; if constexpr (k >= 0) { if constexpr (S.luse[k] == I - 1) asm volatile("" :: "v"(fr[k])); } }
    if constexpr (c0 + 2 < c1) P[c0 + 2] = pk2(D[(c0 + 2) / 2][2 * ((c0 + 2) & 1)], D[(c0 + 2) / 2][2 * ((c0 + 2) & 1) + 1]);
    if constexpr (c0 < c1) P[c0] = pk2(D[c0 / 2][2 * (c0 & 1)], D[c0 / 2][2 * (c0 & 1) + 1]);
    if constexpr (c0 + 1 < c1) P[c0 + 1] = pk2(D[(c0 + 1) / 2][2 * ((c0 + 1) & 1)], D[(c0 + 1) / 2][2 * ((c0 + 1) & 1) + 1]);
    if constexpr (S.tcum[I] < S.tcum[I + 1]) { constexpr int k = 2 * S.tord[S.tcum[I] / 2] + (S.tcum[I] & 1), o = toff(k); T[k] = trd(bt + o); }
    if constexpr (S.tcum[I] + 1 < S.tcum[I + 1]) { constexpr int j = S.tcum[I] + 1, k = 2 * S.tord[j / 2] + (j & 1), o = toff(k); T[k] = trd(bt + o); }
    if constexpr (S.tcum[I] + 2 < S.tcum[I + 1]) { constexpr int j = S.tcum[I] + 2, k = 2 * S.tord[j / 2] + (j & 1), o = toff(k); T[k] = trd(bt + o); }
    static_assert(ld < 0 || lswap(ld, S.nl) < S.lcum[I], "fragment not loaded");
    constexpr int yi = S.yat[0] == I ? 0 : S.yat[1] == I ? 1 : S.yat[2] == I ? 2 : I == S.n - 1 ? 3 : -1;
    if constexpr (yi >= 0) {
      if constexpr (yi == 3) asm volatile("s_nop 7\n\ts_nop 3");
      asm volatile("ds_write_b128 %0, %1 offset:%2\n\tds_write_b32 %3, %0 offset:%4" :: "v"(yaddr), "v"(acc[yi]), "i"(yi * 1024), "v"(faddr), "i"(yi * 16));
    }
    __builtin_amdgcn_sched_barrier(0);
    conv_step<HF, I + 1>(ub, bt, yaddr, faddr, R, fr, T, D, P, acc);
  }
}
template <int HF, int L>
DEVI void conv_pre(u32 ub, bf8 (&fr)[96]) {
  if constexpr (L < PFD) { constexpr int k = lswap(L, 99), o = SchedOf<HF>::v.s.loff[k]; fr[k] = lds_ld<bf8>(ub + o); conv_pre<HF, L + 1>(ub, fr); }
}
template <int HF>
DEVI void conv(u32 ub, u32 bt, u32 yaddr, u32 faddr, const ConvRegs& R) {
  bf8 fr[96]; bf4 T[20]; f4 D[40]; u32 P[80]; f4 acc[4];
  u4v uf;
  asm volatile("ds_read_b128 %0, %1" : "=v"(uf) : "v"(M_UF) : "memory");
  conv_pre<HF, 0>(ub, fr);
  __builtin_amdgcn_sched_barrier(0);
  asm volatile("s_waitcnt lgkmcnt(%1)" : "+v"(uf) : "i"(PFD));
  if (__builtin_expect(__builtin_amdgcn_readfirstlane(uf.x & uf.y & uf.z & uf.w) == 0, 0)) {
    u32 spins = 0;
    do { uf = lds_ldv<u4v>(M_UF); } while ((uf.x & uf.y & uf.z & uf.w) == 0 && ++spins < SPIN_CAP);
    MEMF();
    conv_pre<HF, 0>(ub, fr);
    MEMF();
  }
  __builtin_amdgcn_sched_barrier(0);
  conv_step<HF, 0>(ub, bt, yaddr, faddr, R, fr, T, D, P, acc);
}

#define R_S "+{v[160:163]}"(su0), "+{v[164:167]}"(su1), "+{v[168:171]}"(su2), "+{v[172:175]}"(su3)
#define R_X "+{v[176:179]}"(xu0), "+{v[180:183]}"(xu1), "+{v[184:187]}"(xu2), "+{v[188:191]}"(xu3), "+{v192}"(xk0), "+{v193}"(xk1)
#define LD_S(s0, s1, b0, b1) "global_load_dwordx4 v[160:163], " s0 ", " b0 " sc1\n\tglobal_load_dwordx4 v[164:167], " s0 ", " b1 " sc1\n\t" \
                             "global_load_dwordx4 v[168:171], " s1 ", " b0 " sc1\n\tglobal_load_dwordx4 v[172:175], " s1 ", " b1 " sc1\n\t"
#define LD_X(xo, xko, b0, b1) "global_load_dwordx4 v[176:179], " xo ", " b0 " sc1\n\tglobal_load_dwordx4 v[180:183], " xo ", " b0 " offset:256 sc1\n\t" \
                              "global_load_dwordx4 v[184:187], " xo ", " b1 " sc1\n\tglobal_load_dwordx4 v[188:191], " xo ", " b1 " offset:256 sc1\n\t" \
                              "global_load_dword v192, " xko ", " b0 " sc1\n\tglobal_load_dword v193, " xko ", " b1 " sc1\n\t"
DEVI void spin4(u32 a) {
  u4v f; u32 spins = 0;
  do { f = lds_ldv<u4v>(a); } while (umin(umin(f.x, f.y), umin(f.z, f.w)) == 0 && ++spins < SPIN_CAP);
  MEMF();
}
DEVI u32 spin1(u32 a) {
  u32 f, spins = 0;
  do { f = lds_ldv<u32>(a); } while (f == 0 && ++spins < SPIN_CAP);
  MEMF();
  return f;
}

DEVI void top2_collect(u32& k1, u32& k2) {
  u4v ta_, tb_; u32 spins = 0;
  do { ta_ = lds_ldv<u4v>(M_TOP); tb_ = lds_ldv<u4v>(M_TOP + 16); } while (umin(umin(ta_.x, ta_.z), umin(tb_.x, tb_.z)) == 0 && ++spins < SPIN_CAP);
  const u32 x1 = umax(ta_.x, ta_.z), x2 = umax(umin(ta_.x, ta_.z), umax(ta_.y, ta_.w)), y1 = umax(tb_.x, tb_.z), y2 = umax(umin(tb_.x, tb_.z), umax(tb_.y, tb_.w));
  k1 = umax(x1, y1); k2 = umax(umin(x1, y1), umax(x2, y2));
}

#define SWN "6"
#define XWN "2"
__global__ void __launch_bounds__(512, 2)
s4_moe_kernel(const u16* x_, const u16* Are_, const u16* Aim_, const u16* Bre_, const u16* Bim_, const u16* Cre_,
              const u16* Cim_, const u16* ldt_, const u16* dsk_, const u16* wr_, const u16* wg_, const u16* wu_,
              const u16* wd_, u16* out_, char* ws_, u64 pad_) {
  extern __shared__ __attribute__((aligned(16))) char smem[];
  typedef u64 u64x8 __attribute__((ext_vector_type(8)));
  STAMP_DECL;
  STAMP(0);
  u64x8 ka, kb_; u64 pc;
  asm volatile("s_load_dwordx16 %0, %3, 0x0\n\ts_load_dwordx16 %1, %3, 0x40\n\ts_getpc_b64 %2"
               : "=&s"(ka), "=&s"(kb_), "=&s"(pc) : "s"(__builtin_amdgcn_kernarg_segment_ptr()) : "memory");
  const int lane = threadIdx.x & 63;
  const int wid = __builtin_amdgcn_readfirstlane(threadIdx.x >> 6);
  const int w = blockIdx.x, xc = w & 7, mm = w >> 3;
  u32 pf0;
  asm volatile("s_cmp_lt_u32 %3, 4\n\ts_cbranch_scc1 1f\n\tglobal_load_dword %0, %1, %2\n1:" : "=&v"(pf0)
               : "v"(umin(((lane & 7) * 32 + (wid & 3) * 8 + (mm & 7)) * 128, CODE_BYTES - 128)), "s"(pc), "s"(wid) : "memory", "scc");
  const int cg = xc * 16 + (mm >> 1), hf = mm & 1, cw = wid & 3;
  const int sp = xc * 32 + mm;
  const bool prod = (mm & 1) == 0 && mm < 28;
  const int pi = ssel(prod, 0, 99);
  u4v g0, g1, g2, g3, r0_, r1_, r2_;
  u32 ep, p_ar, p_ai, p_br, p_bi, p_cr, p_ci, p_dt, p_dk;
  u4v c0_, c1_, c2_, c3_, c4_, c5_, c6_, c7_, c8_, c9_;
  const u32 l16 = lane * 16;
  u32 vt = (lane & 7) * 4096 + (lane >> 3) * 16, po = (4 * cg + cw) * 256 + lane * 4, zero = 0;
  {
    const u32 iv = ssel((wid & 6) == 2, SENT, 0u);
    lds_st<u2v>(vsel(wid >= 4, UB + cw * UCH + lane * 8, vsel(wid < 2, MB + (lane & 31) * 8, M_PART + (threadIdx.x & 127) * 8)), (u2v){iv, iv});
  }
  p_dt = 0; p_dk = 0;
  ZS(5, 1);
  asm volatile("s_waitcnt lgkmcnt(0)\n\ts_barrier" : "+s"(ka), "+s"(kb_) :: "memory");
  ZS(5, 2);
  const char* x = (const char*)ka[0];
  const char* wg = (const char*)kb_[2]; const char* wu = (const char*)kb_[3]; const char* wd = (const char*)kb_[4];
  char* out = (char*)kb_[5]; char* ws = (char*)kb_[6];
  {
#define LD_P "global_load_dword %[p0], %[po], %[a0]" PNT "\n\tglobal_load_dword %[p1], %[po], %[a1]" PNT "\n\tglobal_load_dword %[p2], %[po], %[a2]" PNT "\n\t" \
             "global_load_dword %[p3], %[po], %[a3]" PNT "\n\tglobal_load_dword %[p4], %[po], %[a4]" PNT "\n\tglobal_load_dword %[p5], %[po], %[a5]" PNT "\n\t" \
             "global_load_short_d16_hi %[p6], %[dq], %[a6]\n\tglobal_load_short_d16_hi %[p7], %[dq], %[a7]"
    asm volatile("s_cmp_lt_u32 %[wid], 4\n\ts_cbranch_scc0 2f\n\ts_sleep " SLP_A "\n\t"
                 "global_load_dwordx4 %[r0], %[vo], %[xb]" XNT "\n\tglobal_load_dwordx4 %[r1], %[vo], %[xb] offset:1024" XNT "\n\t"
                 "global_load_dword %[ep], %[z], %[ws] sc1\n\tglobal_load_dwordx4 %[r2], %[vo], %[wr]\n2:"
                 : [r0] "=&v"(r0_), [r1] "=&v"(r1_), [r2] "=&v"(r2_), [ep] "=&v"(ep)
                 : [wid] "s"(wid), [z] "v"(zero), [vo] "v"(l16), [xb] "s"(x + (8 * sp + 2 * cw) * 1024), [ws] "s"(ws), [wr] "s"(kb_[1]) : "memory", "scc");
    asm volatile(LD_P : [p0] "=&v"(p_ar), [p1] "=&v"(p_ai), [p2] "=&v"(p_br), [p3] "=&v"(p_bi), [p4] "=&v"(p_cr), [p5] "=&v"(p_ci), [p6] "+v"(p_dt), [p7] "+v"(p_dk)
                 : [dq] "v"((4 * cg + cw) * 2), [a6] "s"(ka[7]), [a7] "s"(kb_[0]), [po] "v"(po), [a0] "s"(ka[1]), [a1] "s"(ka[2]), [a2] "s"(ka[3]), [a3] "s"(ka[4]), [a4] "s"(ka[5]), [a5] "s"(ka[6]) : "memory");
    asm volatile("s_cmp_eq_u32 %[w2], 2\n\ts_cbranch_scc0 2f\n\ts_sleep " SLP_P "\n\t"
                 "global_load_dwordx4 %0, %[vt], %[xt]" XNT "\n\tglobal_load_dwordx4 %1, %[vt], %[xt] offset:1024" XNT "\n\t"
                 "global_load_dwordx4 %2, %[vt], %[xt] offset:2048" XNT "\n\tglobal_load_dwordx4 %3, %[vt], %[xt] offset:3072" XNT "\n2:"
                 : "=&{v[144:147]}"(g0), "=&{v[148:151]}"(g1), "=&{v[152:155]}"(g2), "=&{v[156:159]}"(g3)
                 : [w2] "s"(wid & 6), [vt] "v"(vt), [xt] "s"(x + (sp >> 3) * 65536 + (sp & 7) * 128 + 32768 * (cw & 1)) : "memory", "scc");
  }
  ZS(5, 3);
#define NPF 1
  ZS(5, 4);
  STAMP(14);

  const u32 rb = (wid ^ 4) * KREG;
  const int hh = lane >> 5, j = lane & 31;
  u32 fa = rb + j * TROW + hh * 16;
  asm volatile("s_mov_b32 m0, %0" :: "s"(rb));
  f16v kd = {};
  u32 lc = M_LC + l16, zA = rb + O_TA + umin(lane, 52) * 16;
  PIN(V(fa), V(kd), V(lc), V(zA));
  ZS(5, 5);
  const int tA_ = cw + (cw & 2) * 2, tB_ = cw + 2, tile = ssel(hf, tB_, tA_), tilo = ssel(hf, tA_, tB_);
  u32 tag, chs, so0 = W_ST + (lane & 1) * 8192 + (lane >> 1) * 16 + tile * 512, so1 = so0 + (tilo - tile) * 512;
  u32 xo = W_XB + (lane >> 4) * 8 * XBS + (lane & 15) * 16 + (mm >> 1) * 512, xko = W_XB + 8192 + (lane >> 4) * 8 * XBS + (lane & 15) * 8 + (mm >> 2) * 128 + ((mm >> 1) & 1) * 4;
  PIN(V(so0), V(so1), V(xo), V(xko));

  if (wid < 4) {
    __builtin_amdgcn_s_setprio(3);
    float s0 = 0, q0 = 0, d0 = 0, s1 = 0, q1 = 0, d1 = 0, sw = 0;
    u32 sto = lane == 31 ? cw * 4096 + sp * 16 : 0x7ffffff0u;
    const __amdgpu_buffer_rsrc_t rs = __builtin_amdgcn_make_buffer_rsrc(ws + W_ST, 0, 16384, 0x00020000);
    PIN(V(s0), V(q0), V(d0), V(s1), V(q1), V(d1), V(sw), V(sto));
    u32 o = (lane >> 3) * 1024 + (cw & 1) * 128 + (lane & 7) * 16, oc = 8192 + (lane >> 3) * 128 + (cw & 1) * 64 + (lane & 7) * 8;
    PIN(V(o), V(oc));
    asm volatile("s_cmp_lt_u32 %4, 2\n\ts_cbranch_scc0 1f\n\ts_waitcnt vmcnt(8)\n1:\n\ts_waitcnt vmcnt(12)" : V(ep), V(r0_), V(r1_), V(r2_) : "s"(wid) : "memory", "scc");
    STAMP(6); ZS(5, 6);
    tag = __builtin_amdgcn_readfirstlane(ep) + 1u;
    chs = tag + tag - 1u;
    lds_st<u2v>(cw == 0 ? M_TAG : M_DUM, (u2v){chs, tag});
    const u32 ra[4] = {r0_.x, r0_.y, r0_.z, r0_.w}, rc[4] = {r1_.x, r1_.y, r1_.z, r1_.w}, wv[4] = {r2_.x, r2_.y, r2_.z, r2_.w};
#pragma unroll
    for (int i = 0; i < 4; i++) {
      s0 = dot2(ra[i], 0x3F803F80u, s0); q0 = dot2(ra[i], ra[i], q0); d0 = dot2(ra[i], wv[i], d0); sw = dot2(wv[i], 0x3F803F80u, sw);
      s1 = dot2(rc[i], 0x3F803F80u, s1); q1 = dot2(rc[i], rc[i], q1); d1 = dot2(rc[i], wv[i], d1);
    }
    float sx;
#define D4(ctl) "v_add_f32_dpp %0, %0, %0 " ctl "\n\tv_add_f32_dpp %2, %2, %2 " ctl "\n\tv_add_f32_dpp %4, %4, %4 " ctl "\n\tv_add_f32_dpp %6, %6, %6 " ctl "\n\t"
    asm volatile("s_nop 2\n\tv_mov_b32 %7, %6\n\tv_permlane32_swap_b32 %0, %1\n\tv_permlane32_swap_b32 %2, %3\n\tv_permlane32_swap_b32 %4, %5\n\t"
                 "v_permlane32_swap_b32 %6, %7\n\tv_add_f32 %0, %0, %1\n\tv_add_f32 %2, %2, %3\n\tv_add_f32 %4, %4, %5\n\tv_add_f32 %6, %6, %7\n\t"
                 D4("row_ror:8 row_mask:0xf bank_mask:0xf") D4("row_ror:4 row_mask:0xf bank_mask:0xf") D4("row_ror:2 row_mask:0xf bank_mask:0xf")
                 D4("row_ror:1 row_mask:0xf bank_mask:0xf") D4("row_bcast:15 row_mask:0xa bank_mask:0xf") ""
                 : V(s0), V(s1), V(q0), V(q1), V(d0), V(d1), V(sw), "=&v"(sx));
    const float mean = s0 * (1.f / 512.f);
    const float var = fmaxf(q0 * (1.f / 512.f) - mean * mean, 0.f);
    const u32 ms = pk2(mean, __builtin_amdgcn_sqrtf(rbf(rbf(var) + 1e-5f)));
    const float r = (d0 - bl(ms) * sw) * __builtin_amdgcn_rcpf(bh(ms));
    const u32 rbits = pk2(r, r), ok = rbits ^ ((u32)((int)rbits >> 31) | 0x80000000u);
    const auto e0 = __builtin_amdgcn_permlane32_swap(ms, ms, false, false);
    const auto e1 = __builtin_amdgcn_permlane32_swap(ok, ok, false, false);
    const u32 rr = __builtin_amdgcn_perm(e1[1], e1[0], 0x07060302u);
    __builtin_amdgcn_raw_buffer_store_b128((u4v){e0[0], e0[1], rr, chs - (e0[0] + e0[1] + rr)}, rs, sto, 0, 16);
    ZS(5, 8);
    if (cw >= 2) {
      const __amdgpu_buffer_rsrc_t rt = __builtin_amdgcn_make_buffer_rsrc(ws + W_XB + sp * XBS, 0, XBS, 0x00020000);
      asm volatile("s_waitcnt vmcnt(1)\n\tv_swap_b32 v146, v148\n\tv_swap_b32 v147, v149\n\tv_swap_b32 v154, v156\n\tv_swap_b32 v155, v157"
                   : "+{v[144:147]}"(g0), "+{v[148:151]}"(g1), "+{v[152:155]}"(g2), "+{v[156:159]}"(g3) :: "memory");
      __builtin_amdgcn_raw_buffer_store_b128(g0, rt, o, 0, 16);
      __builtin_amdgcn_raw_buffer_store_b128(g2, rt, o + 256, 0, 16);
      __builtin_amdgcn_raw_buffer_store_b128(g1, rt, o + 512, 0, 16);
      __builtin_amdgcn_raw_buffer_store_b128(g3, rt, o + 768, 0, 16);
      const u32 ca = g0.x + g0.y + g0.z + g0.w + g2.x + g2.y + g2.z + g2.w, cb = g1.x + g1.y + g1.z + g1.w + g3.x + g3.y + g3.z + g3.w;
      __builtin_amdgcn_raw_buffer_store_b64((u2v){chs - ca, chs - cb}, rt, oc, 0, 16);
    }
    __builtin_amdgcn_s_setprio(PRIO_C);
    asm volatile("s_mov_b32 m0, %[d]\n\ts_nop 0\n\tglobal_load_lds_dwordx4 %[vo], %[lt]\n\tglobal_load_lds_dwordx4 %[vo], %[lt] offset:1024\n\t"
                 "global_load_lds_dwordx4 %[vo], %[lt] offset:2048\n\tglobal_load_lds_dwordx4 %[vo], %[lt] offset:3072\n\ts_mov_b32 m0, %[r]\n\t"
                 "s_cmp_lt_u32 %[wid], 2\n\ts_cbranch_scc0 1f\n\ts_waitcnt vmcnt(5)\n1:"
                 :: [vo] "v"(l16), [lt] "s"((const char*)&LT + cw * 4096), [d] "s"(M_LC + cw * 4096), [r] "s"(rb), [wid] "s"(wid) : "memory", "scc");
  }
  asm volatile("s_cmp_lt_u32 %1, 4\n\ts_cbranch_scc1 1f\n\ts_setprio " PRIO_KS "\n\ts_waitcnt vmcnt(0)\n1:" : V(pf0) : "s"(wid) : "memory", "scc");
  ZS(5, 9);
  PIN(V(p_dt), V(p_dk), V(p_ar), V(p_ai), V(p_br), V(p_bi), V(p_cr), V(p_ci));
  float dt = rbf(__builtin_amdgcn_exp2f(__uint_as_float(p_dt) * 1.44269504f));
  float dl = dt * 1.44269504f, dr = dt * 0.159154943f;

  STAMP(1);
  ZS(3, 1); ZS(5, 10);

  u4v su0, su1, su2, su3, xu0, xu1, xu2, xu3; u32 xk0, xk1;
  const char* xb0 = ws + (tile * 32 + xc) * XBS; const char* xb1 = ws + (tilo * 32 + xc) * XBS;
  {
    const u32 sel = ssel(wid < 4, 0x01000c0cu, 0x03020c0cu);
#define UP(p) __uint_as_float(__builtin_amdgcn_perm(0u, p, sel))
    const float a2 = dl * UP(p_ar), rv = dr * UP(p_ai);
    const f2 Bc = {dt * UP(p_br), dt * UP(p_bi)}, Cc = {UP(p_cr), UP(p_ci)};
    const f2 G = cmul(Cc, Bc, rot(Bc));
    const f2 w1 = cexp(a2, rv, 1.f), w256 = cexp(a2, rv, 256.f);
    const f2 w1r = rot(w1), w256r = rot(w256);
    const f2 w2 = cmul(w1, w1, w1r), w2r = rot(w2), w4 = cmul(w2, w2, w2r), w4r = rot(w4);
    const f2 v8 = {fmaf(w4.x, w4.x, w4r.x * w4.y), (w4.x + w4.x) * w4r.x};
    const f2 v16 = {fmaf(v8.x, v8.x, -v8.y * v8.y), (v8.x + v8.x) * v8.y}, v16r = rot(v16);
#define TW(r, v) asm volatile("ds_write_addtid_b32 %0 offset:%1" :: "v"(pk2((v).x, (v).y)), "i"((r) * TROW))
    ZS(3, 2);
    f2 fz = G, ge = v16, go = v8, fh = cmul(G, w256, w256r);
    TW(0, fz); TW(21, ((f2){1.f, 0.f})); TW(22, go); TW(23, ge); TW(8, fh);
    f2 t2 = fh;
#pragma unroll
    for (int i = 1; i < 16; i++) {
      go = cmul(go, v16, v16r);
      if (i < 15) ge = cmul(ge, v16, v16r);
      f2 t4;
      if (i < 8) fz = cmul(fz, w1, w1r); else if (i < 15) fh = cmul(fh, w256, w256r);
      if (i >= 11 && i < 14) t2 = cmul(t2, w2, w2r);
      if (i == 8 || i == 9) t4 = cmul(fh, w4, w4r);
      TW(22 + 2 * i, go);
      if (i < 15) TW(23 + 2 * i, ge);
      if (i < 8) TW(i, fz); else if (i < 10) { TW(12 + (i - 8), fh); TW(14 + (i - 8), t4); } else if (i < 15) TW(16 + (i - 10), fh);
      if (i >= 11 && i < 14) TW(9 + (i - 11), t2);
      if (i == 1) ZS(3, 3); if (i == 4) ZS(3, 4); if (i == 7) ZS(3, 5); if (i == 11) ZS(3, 6); if (i == 15) ZS(3, 7);
      if (i == S_AT)
        asm volatile("s_cmp_lt_u32 %8, 4\n\ts_cbranch_scc0 1f\n\t" LD_S("%4", "%5", "%6", "%7") "1:"
                     : "=&{v[160:163]}"(su0), "=&{v[164:167]}"(su1), "=&{v[168:171]}"(su2), "=&{v[172:175]}"(su3)
                     : "v"(so0), "v"(so1), "s"(ws), "s"(ws + 4096), "s"(wid) : "scc");
      if (i == X_AT)
        asm volatile("s_cmp_lt_u32 %10, 4\n\ts_cbranch_scc0 1f\n\t" LD_X("%6", "%7", "%8", "%9") "1:"
                     : "=&{v[176:179]}"(xu0), "=&{v[180:183]}"(xu1), "=&{v[184:187]}"(xu2), "=&{v[188:191]}"(xu3), "=&{v192}"(xk0), "=&{v193}"(xk1)
                     : "v"(xo), "v"(xko), "s"(xb0), "s"(xb1), "s"(wid) : "scc");
      __builtin_amdgcn_sched_barrier(0);
    }
  }
  MEMF();
  ZS(3, 8);

  if (wid >= 4) {
    kd[0] = lane == 0 ? __uint_as_float(p_dk) : 0.f;
    bf8 tf[16], tg[16];
    const u32 fb = fa + CB;
#pragma unroll
    for (int s = 0; s < 8; s++) { tf[2 * s] = lds_ld<bf8>(fa + 32 * s); tf[2 * s + 1] = lds_ld<bf8>(fa + 21 * TROW + 32 * s); }
    MEMF();
    u4v tfl;
    asm volatile("ds_read_b128 %0, %1" : "=v"(tfl) : "v"(M_TF) : "memory");
    __builtin_amdgcn_sched_barrier(0);
    __builtin_amdgcn_s_setprio(2);
    ZS(3, 9);
#pragma unroll
    for (int s = 0; s < 8; s++) {
      kd = __builtin_amdgcn_mfma_f32_32x32x16_bf16(tf[2 * s], tf[2 * s + 1], kd, 0, 0, 0);
      __builtin_amdgcn_sched_barrier(0);
      tg[2 * s] = lds_ld<bf8>(fb + 32 * s); tg[2 * s + 1] = lds_ld<bf8>(fb + 21 * TROW + 32 * s);
      if (s == 7) lds_st<u4v>(zA, (u4v){0, 0, 0, 0});
      __builtin_amdgcn_sched_barrier(0);
    }
    asm volatile("s_waitcnt lgkmcnt(15)" : V(tfl));
    STAMP(2); ZS(3, 10);
    if (__builtin_expect(__builtin_amdgcn_readfirstlane(tfl.x & tfl.y & tfl.z & tfl.w) == 0, 0)) {
      spin4(M_TF);
#pragma unroll
      for (int s = 0; s < 8; s++) { tg[2 * s] = lds_ld<bf8>(fb + 32 * s); tg[2 * s + 1] = lds_ld<bf8>(fb + 21 * TROW + 32 * s); }
      MEMF();
    }
    u32 fl = M_YF + cw * 4;
    PIN(V(fl));
    asm volatile("ds_write_b32 %0, %0 offset:%1" :: "v"(fl), "i"(M_RF - M_YF) : "memory");
    STAMP(7); ZS(3, 11);
    c6_ = lds_ld<u4v>(lc + 6144); c7_ = lds_ld<u4v>(lc + 7168); c9_ = lds_ld<u4v>(lc + 9216); c8_ = lds_ld<u4v>(lc + 8192); c5_ = lds_ld<u4v>(lc + 5120);
    ConvRegs CR;
#pragma unroll
    for (int i = 0; i < 5; i++) CR.bands[i] = lds_ld<bf8>(lc + i * 1024);
    __builtin_amdgcn_sched_barrier(0);
#pragma unroll
    for (int s = 0; s < 8; s++) kd = __builtin_amdgcn_mfma_f32_32x32x16_bf16(tg[2 * s], tg[2 * s + 1], kd, 0, 0, 0);
    u32 a0 = rb + c6_.x, a1 = rb + c6_.y, tA = rb + c6_.z, tB = rb + c6_.w, sa = rb + c7_.x, pA = rb + c7_.z, cA2 = rb + c7_.w,
        bn = rb + c8_.x, bt = rb + c8_.y, ya = rb + c8_.z, ub = cw * UCH + c8_.w, cA = rb + l16;
    PIN(V(a0), V(a1), V(tA), V(tB), V(sa), V(pA), V(cA2), V(bn), V(bt), V(ya), V(ub), V(cA));
    ZS(2, 1); ZS(3, 12);
    {
      lds_st<u2v>(a0, (u2v){pk2(kd[0], kd[1]), pk2(kd[2], kd[3])});
      const float en = DPPF(kd[4], 0x130, 0xf);
      const u32 e00 = pk2(kd[4], kd[4]);
      lds_st<u4v>(a1, (u4v){e00 & c9_.x, (pk2(kd[5], kd[6]) & c9_.y) | (e00 & c9_.z), pk2(kd[6], kd[6]) & c9_.y, pk2(kd[7], en) & c9_.y});
      ZS(2, 2);
      const u4v c = lds_ld<u4v>(cA);
      const u2v p = lds_ld<u2v>(pA);
      const u32 v01 = hh ? pk2(kd[4], kd[5]) : pk2(kd[8], kd[9]), v23 = hh ? pk2(kd[6], kd[7]) : pk2(kd[10], kd[11]);
      lds_st<u16>(tA, (u16)v01); lds_st<u16>(tA + 32, (u16)(v01 >> 16));
      lds_st<u16>(tB, (u16)v23); lds_st<u16>(tB + 32, (u16)(v23 >> 16));
      const u32 k8 = pk2(kd[8], kd[8]);
      const u32 al = rl(k8, 0), k2048 = rl(k8, 32);
      lds_st<u16>(sa, (u16)(lane < 2 ? 0u : lane < 4 ? al : k2048));
      ZS(2, 3);
      const u4v c2 = lds_ld<u4v>(cA2);
      const u2v p2 = lds_ld<u2v>(cA2 - 8);
      CR.An[17] = __builtin_bit_cast(bf8, c5_ & (al ^ 0x80008000u));
#define SHIFTS(c, p, A) do { \
      const u32 s0 = __builtin_amdgcn_alignbit(p.y, p.x, 16), s1 = __builtin_amdgcn_alignbit(c.x, p.y, 16), s2 = __builtin_amdgcn_alignbit(c.y, c.x, 16), \
                s3 = __builtin_amdgcn_alignbit(c.z, c.y, 16), s4 = __builtin_amdgcn_alignbit(c.w, c.z, 16); \
      lds_st<u4v>(A + CS, (u4v){s1, s2, s3, s4}); lds_st<u4v>(A + 2 * CS, (u4v){p.y, c.x, c.y, c.z}); lds_st<u4v>(A + 3 * CS, (u4v){s0, s1, s2, s3}); } while (0)
      SHIFTS(c, p, cA);
      ZS(2, 4);
      SHIFTS(c2, p2, cA2);
      ZS(2, 5);
    }
    MEMF();
#pragma unroll
    for (int m = 0; m < 17; m++) CR.An[m] = cat(trd(bn + 64 * m + 8), trd(bn + 64 * m));
    ZS(2, 7);
    STAMP(3);
    ZS(2, 8);
    STAMP(4);
    if (hf == 0) conv<0>(ub, bt, ya, fl, CR); else conv<1>(ub, bt, ya, fl, CR);
    STAMP(5); ZS(4, 1);
    if (cw == 0) {
      if (spin1(M_AF) == 1) {
        const u4v fr = lds_ld<u4v>(M_FR + l16);
        const u32 tag = __builtin_amdgcn_readfirstlane(lds_ld<u32>(M_TAG + 4));
        spin4(M_YF + 48);
        float se = rbf(lds_ld<float>(fr.z));
        const float rsg = __uint_as_float(fr.x);
        const u32 ue = fr.y, xe = fr.y >> 16, st_o = fr.w;
        PIN(V(se));
        u4v h0, h1, h2, h3;
        ZS(4, 3);
        {
          u32 n_, t0_, t1_;
          const u64 fbp = (u64)(ws + W_FB + cg * 32);
          const u4v rf = {(u32)fbp, (u32)(fbp >> 32), 112 * 4096, 0x00020000};
          const u32 fo = (lane >> 1) * 4096 + (lane & 1) * 16;
          if (prod) __builtin_amdgcn_s_sleep(SLP_FP); else __builtin_amdgcn_s_sleep(SLP_F);
          asm volatile("s_movk_i32 %[n], 0x4000\n0:\n\t" PASS_B "s_waitcnt vmcnt(0)\n\t" CHK_B "s_cbranch_vccz 2f\n\ts_sleep " SLP_L "\n\ts_sub_u32 %[n], %[n], 1\n\ts_cbranch_scc0 0b\n2:"
                       : "=&{v[160:163]}"(h0), "=&{v[164:167]}"(h1), "=&{v[168:171]}"(h2), "=&{v[172:175]}"(h3), [a] "=&v"(t0_), [c] "=&v"(t1_), [n] "=&s"(n_)
                       : [fo] "v"(fo), [f3] "v"(fo & 0xffffu), [rf] "s"(rf), [k] "s"(tag), [s1] "s"(131072), [s2] "s"(2 * 131072), [s3] "s"(3 * 131072) : "memory", "scc", "vcc");
        }
        ZS(4, 4);
#define AF(v) __uint_as_float(v)
        if (lane >= 32) { h3.x = 0; h3.z = 0; }
        float v0 = (bl(h0.x) + bl(h1.x)) + (bl(h2.x) + bl(h3.x)), v1 = (bh(h0.x) + bh(h1.x)) + (bh(h2.x) + bh(h3.x));
        float v2 = (bl(h0.z) + bl(h1.z)) + (bl(h2.z) + bl(h3.z)), v3 = (bh(h0.z) + bh(h1.z)) + (bh(h2.z) + bh(h3.z));
        const auto a = __builtin_amdgcn_permlane16_swap(__float_as_uint(v0), __float_as_uint(v1), false, false);
        const auto a2 = __builtin_amdgcn_permlane16_swap(__float_as_uint(v2), __float_as_uint(v3), false, false);
        v0 = AF(a[0]) + AF(a[1]); v2 = AF(a2[0]) + AF(a2[1]);
        const auto b = __builtin_amdgcn_permlane32_swap(__float_as_uint(v0), __float_as_uint(v2), false, false);
        v0 = AF(b[0]) + AF(b[1]);
        v0 += DPPF(v0, 0x128, 0xf); v0 += DPPF(v0, 0x124, 0xf); v0 += DPPF(v0, 0x122, 0xf);
        const float bb = rbf(bl(ue) + rbf(rbf(v0) * rsg));
        const float oe = bl(xe) + rbf(se * rbf(bb * sigm(bb)));
        __builtin_amdgcn_raw_buffer_store_b16((u16)pk2(oe, oe), __builtin_amdgcn_make_buffer_rsrc(out + cg * 8, 0, 2048 * 1024 - 1024 + 8, 0x00020000), st_o, 0, 0);
        ZS(4, 5);
        STAMP(13);
      }
    }
    STAMP_FLUSH();
    return;
  }

  asm volatile("s_waitcnt vmcnt(10)" ::: "memory");
  lds_stv<u32>(M_TF + cw * 4, 1u);
  __builtin_amdgcn_s_setprio(PRIO_S);
  u32 pfw = 0;
  STAMP(2);
  const int fp = xc * 14 + (mm >> 1);
  u32 uw0 = UB + 512 + 8 * lane + tile * 512, uw1 = UB + 512 + 8 * lane + tilo * 512, ib0 = 0xffffu - (256 * tile + 4 * lane), ib1 = ib0 - (tilo - tile) * 256, tw = lane == 63 ? M_TOP + cw * 8 : M_DUM;
  u4v xs0, xs1, xs2, xs3; u32 msel;
  xs0 = xs1 = xs2 = xs3 = (u4v){0x3F803F80u, 0x3F803F80u, 0x3F803F80u, 0x3F803F80u};
  PIN(V(uw0), V(uw1), V(ib0), V(ib1), V(tw), "+{v[194:197]}"(xs0), "+{v[198:201]}"(xs1), "+{v[202:205]}"(xs2), "+{v[206:209]}"(xs3));
  {
    u32 t0_, t1_, t2_, t3_, n_;
    asm volatile("s_cmp_lt_u32 %[mm], 14\n\ts_cbranch_scc0 1f\n\ts_waitcnt vmcnt(6)\n1:\n\ts_waitcnt vmcnt(" SWN ")\n\ts_movk_i32 %[n], 0x4000\n"
                 "0:\n\tv_add3_u32 %[a], v160, v161, v162\n\tv_add3_u32 %[b], v164, v165, v166\n\tv_add3_u32 %[c], v168, v169, v170\n\tv_add3_u32 %[d], v172, v173, v174\n\t"
                 "v_add3_u32 %[a], %[a], v163, %[k]\n\tv_add3_u32 %[b], %[b], v167, %[k]\n\tv_add3_u32 %[c], %[c], v171, %[k]\n\tv_add3_u32 %[d], %[d], v175, %[k]\n\t"
                 "v_or3_b32 %[a], %[a], %[b], %[c]\n\tv_or_b32 %[a], %[a], %[d]\n\tv_cmp_ne_u32 vcc, 0, %[a]\n\ts_cbranch_vccz 2f\n\t"
                 "s_sub_u32 %[n], %[n], 1\n\ts_cbranch_scc1 2f\n\ts_sleep 2\n\t" LD_S("%[s0]", "%[s1]", "%[b0]", "%[b1]") "s_waitcnt vmcnt(0)\n\ts_branch 0b\n2:"
                 : R_S, [a] "=&v"(t0_), [b] "=&v"(t1_), [c] "=&v"(t2_), [d] "=&v"(t3_), [n] "=&s"(n_)
                 : [mm] "s"(pi), [k] "s"(0u - chs), [s0] "v"(so0), [s1] "v"(so1), [b0] "s"(ws), [b1] "s"(ws + 4096) : "memory", "scc", "vcc");
  }
  STAMP(3);
  if (!prod) {
    const u32 wi = ((mm & 1) ? (mm >> 1) : 2 + (mm >> 1)) * 4 + cw, id = wi * 64 + lane;
    const u32 a = umin(id, 4095), r = a >> 2;
    asm volatile("global_load_dword %0, %1, %2" WNT : "=&v"(pfw) : "v"((r & 511) * 3584 + ((xc * 448) & ~127) + (a & 3) * 128), "s"(wi < 32 ? wg : wu) : "memory");
    asm volatile("global_load_dword %0, %1, %2" WNT : "+v"(pfw) : "v"((xc * 1792 + wi * 25 + umin(lane, 24) - (wi == 71 ? 8 : 0)) * 128), "s"(wd) : "memory");
  }
  u32 k1, k2;
  {
    const u32 ka0 = (su0.z << 16) | ib0, ka1 = (su0.z & 0xffff0000u) | (ib0 - 1);
    k1 = umax(ka0, ka1); k2 = umin(ka0, ka1);
#define INS(k) do { const u32 k_ = (k); k2 = umed3(k1, k2, k_); k1 = umax(k1, k_); } while (0)
    INS((su1.z << 16) | (ib0 - 2)); INS((su1.z & 0xffff0000u) | (ib0 - 3)); INS((su2.z << 16) | ib1); INS((su2.z & 0xffff0000u) | (ib1 - 1));
    INS((su3.z << 16) | (ib1 - 2)); INS((su3.z & 0xffff0000u) | (ib1 - 3));
    u32 t;
#define M2(ctl) "v_min_u32_dpp %2, %0, %0 " ctl "\n\tv_max_u32_dpp %0, %0, %0 " ctl "\n\tv_max_u32_dpp %1, %1, %1 " ctl "\n\tv_max_u32 %1, %1, %2\n\t"
    asm volatile("s_nop 1\n\t" M2("row_ror:8 row_mask:0xf bank_mask:0xf") M2("row_ror:4 row_mask:0xf bank_mask:0xf") M2("row_ror:2 row_mask:0xf bank_mask:0xf")
                 M2("row_ror:1 row_mask:0xf bank_mask:0xf") M2("row_bcast:15 row_mask:0xa bank_mask:0xf") M2("row_bcast:31 row_mask:0xc bank_mask:0xf") ""
                 : V(k1), V(k2), "=&v"(t));
    lds_stv<u2v>(tw, (u2v){k1, k2});
  }
  f2 na[4], nb[4];
  {
    const u32 msv[8] = {su0.x, su0.y, su1.x, su1.y, su2.x, su2.y, su3.x, su3.y};
#pragma unroll
    for (int i = 0; i < 4; i++) {
      na[i] = (f2){__builtin_amdgcn_rcpf(bh(msv[2 * i])), __builtin_amdgcn_rcpf(bh(msv[2 * i + 1]))};
      nb[i] = -(f2){bl(msv[2 * i]), bl(msv[2 * i + 1])} * na[i];
    }
  }
  PIN(V(na[0]), V(na[1]), V(na[2]), V(na[3]), V(nb[0]), V(nb[1]), V(nb[2]), V(nb[3]));
  u32 xe;
#define XE_LOAD() asm volatile("global_load_ushort v211, %1, %2" : "=&{v211}"(xe) : "v"((~((lane & 1) ? k2 : k1) & 2047) * 1024 + (lane >> 4) * 2), "s"(x + cg * 8) : "memory")
  STAMP(4);
  if (prod) {
    top2_collect(k1, k2);
    const u32 ts = ~((lane & 1) ? k2 : k1) & 2047;
    u64 sv;
    XE_LOAD();
    asm volatile("global_load_dword v210, %8, %9 sc1\n\ts_mov_b64 %5, exec\n\ts_mov_b64 exec, %10\n\t"
                 "global_load_dwordx4 v[194:197], %6, %7\n\tglobal_load_dwordx4 v[198:201], %6, %7 offset:64\n\t"
                 "global_load_dwordx4 v[202:205], %6, %7 offset:128\n\tglobal_load_dwordx4 v[206:209], %6, %7 offset:192\n\ts_mov_b64 exec, %5"
                 : "+{v[194:197]}"(xs0), "+{v[198:201]}"(xs1), "+{v[202:205]}"(xs2), "+{v[206:209]}"(xs3), "=&{v210}"(msel), "=&s"(sv)
                 : "v"(ts * 1024 + 256 * cw + (lane & 48)), "s"(x), "v"(W_ST + ((ts >> 1) & 3) * 4096 + (ts >> 3) * 16 + (ts & 1) * 4), "s"(ws),
                   "s"(0x0003000300030003ull) : "memory");
  }
  STAMP(5);
  {
    u32 t0_, t1_, n_;
    asm volatile("s_cmp_lt_u32 %[mm], 14\n\ts_cbranch_scc1 1f\n\ts_waitcnt vmcnt(" XWN ")\n1:\n\ts_waitcnt vmcnt(6)\n\ts_movk_i32 %[n], 0x4000\n"
                 "0:\n\tv_add3_u32 %[a], v176, v177, v178\n\tv_add3_u32 %[b], v184, v185, v186\n\tv_add3_u32 %[a], %[a], v179, v180\n\tv_add3_u32 %[b], %[b], v187, v188\n\t"
                 "v_add3_u32 %[a], %[a], v181, v182\n\tv_add3_u32 %[b], %[b], v189, v190\n\tv_add3_u32 %[a], %[a], v183, v192\n\tv_add3_u32 %[b], %[b], v191, v193\n\t"
                 "v_bitop3_b32 %[a], %[a], %[b], %[k] bitop3:0x7e\n\tv_cmp_ne_u32 vcc, 0, %[a]\n\ts_cbranch_vccz 2f\n\t"
                 "s_sub_u32 %[n], %[n], 1\n\ts_cbranch_scc1 2f\n\ts_sleep 2\n\t" LD_X("%[xo]", "%[xko]", "%[b0]", "%[b1]") "s_waitcnt vmcnt(0)\n\ts_branch 0b\n2:"
                 : R_X, [a] "=&v"(t0_), [b] "=&v"(t1_), [n] "=&s"(n_)
                 : [mm] "s"(pi), [k] "s"(chs), [xo] "v"(xo), [xko] "v"(xko), [b0] "s"(xb0), [b1] "s"(xb1) : "memory", "scc", "vcc");
  }
  STAMP(7);
  f2 xp[4][2], up[4][2];
  {
    const u4v xq[4] = {xu0, xu1, xu2, xu3};
    u32 pk[4][4];
#pragma unroll
    for (int i = 0; i < 4; i++) {
      const f2 v[4] = {{bl(xq[i].x), bl(xq[i].z)}, {bh(xq[i].x), bh(xq[i].z)}, {bl(xq[i].y), bl(xq[i].w)}, {bh(xq[i].y), bh(xq[i].w)}};
#pragma unroll
      for (int c = 0; c < 4; c++) {
        const f2 u = __builtin_elementwise_fma(v[c], na[i], nb[i]);
        pk[c][i] = pk2(u.x, u.y);
        if (i < 2) { xp[c][i] = v[c]; up[c][i] = u; }
      }
    }
#pragma unroll
    for (int c = 0; c < 4; c++) { lds_st<u2v>(uw0 + c * UCH, (u2v){pk[c][0], pk[c][1]}); lds_st<u2v>(uw1 + c * UCH, (u2v){pk[c][2], pk[c][3]}); }
  }
  MEMF();
  lds_stv<u32>(M_UF + cw * 4, 1u);
  MEMF();
  STAMP(8);
  if (prod) {
    c0_ = lds_ld<u4v>(lc + 10240); c1_ = lds_ld<u4v>(lc + 11264);
    spin1(M_RF + cw * 4);
    const u32 go = c0_.w + 128 * cw * 3584 + fp * 32;
    constexpr u32 GS = 32 * 3584 - 1024;
    const u32 da = c1_.x + 16 * fp * 1024 + 256 * cw, db = c1_.y + 16 * fp * 1024 + 256 * cw;
    dma16x4(go, go + GS, go + 2 * GS, go + 3 * GS, wg, rb);
    dma16x4(go, go + GS, go + 2 * GS, go + 3 * GS, wu, rb + 4096);
    dma16x4(da, db + 3072, da + 6144, db + 9216, wd, rb + 8192);
    c2_ = lds_ld<u4v>(lc + 12288); c3_ = lds_ld<u4v>(lc + 13312); c4_ = lds_ld<u4v>(lc + 14336); c5_ = lds_ld<u4v>(lc + 15360);
  }
  float gt[4][4];
  auto silu = [&]() {
#pragma unroll
    for (int c = 0; c < 4; c += 2)
      asm volatile("v_mul_f32 %0, 0xbfb8aa3b, %8\n\tv_mul_f32 %1, 0xbfb8aa3b, %9\n\tv_mul_f32 %2, 0xbfb8aa3b, %10\n\tv_mul_f32 %3, 0xbfb8aa3b, %11\n\tv_mul_f32 %4, 0xbfb8aa3b, %12\n\tv_mul_f32 %5, 0xbfb8aa3b, %13\n\tv_mul_f32 %6, 0xbfb8aa3b, %14\n\tv_mul_f32 %7, 0xbfb8aa3b, %15\n\tv_exp_f32 %0, %0\n\tv_exp_f32 %1, %1\n\tv_exp_f32 %2, %2\n\tv_exp_f32 %3, %3\n\tv_exp_f32 %4, %4\n\tv_exp_f32 %5, %5\n\tv_exp_f32 %6, %6\n\tv_exp_f32 %7, %7\n\tv_add_f32 %0, 1.0, %0\n\tv_add_f32 %1, 1.0, %1\n\tv_add_f32 %2, 1.0, %2\n\tv_add_f32 %3, 1.0, %3\n\tv_add_f32 %4, 1.0, %4\n\tv_add_f32 %5, 1.0, %5\n\tv_add_f32 %6, 1.0, %6\n\tv_add_f32 %7, 1.0, %7\n\tv_rcp_f32 %0, %0\n\tv_rcp_f32 %1, %1\n\tv_rcp_f32 %2, %2\n\tv_rcp_f32 %3, %3\n\tv_rcp_f32 %4, %4\n\tv_rcp_f32 %5, %5\n\tv_rcp_f32 %6, %6\n\tv_rcp_f32 %7, %7\n\tv_mul_f32 %0, %0, %8\n\tv_mul_f32 %1, %1, %9\n\tv_mul_f32 %2, %2, %10\n\tv_mul_f32 %3, %3, %11\n\tv_mul_f32 %4, %4, %12\n\tv_mul_f32 %5, %5, %13\n\tv_mul_f32 %6, %6, %14\n\tv_mul_f32 %7, %7, %15"
                   : "=&v"(gt[c][0]), "=&v"(gt[c][1]), "=&v"(gt[c][2]), "=&v"(gt[c][3]), "=&v"(gt[c + 1][0]), "=&v"(gt[c + 1][1]), "=&v"(gt[c + 1][2]), "=&v"(gt[c + 1][3])
                   : "v"(up[c][0].x), "v"(up[c][0].y), "v"(up[c][1].x), "v"(up[c][1].y), "v"(up[c + 1][0].x), "v"(up[c + 1][0].y), "v"(up[c + 1][1].x), "v"(up[c + 1][1].y));
  };
  silu();
  STAMP(11);
  if (!prod) {
    top2_collect(k1, k2);
    XE_LOAD();
    if (w == 255 && cw == 0 && lane == 0) __hip_atomic_store((GAS u32*)(ws + W_EP), tag, __ATOMIC_RELAXED, __HIP_MEMORY_SCOPE_AGENT);
  }

  auto epi = [&]() {
  const u32 t0 = ~k1 & 2047, t1 = ~k2 & 2047, tr = 256 * tile + 4 * lane;
  u32 yo = O_Y + cw * 1024 + l16, oo[4];
#pragma unroll
  for (int r = 0; r < 4; r++) { const u32 rr = r ^ (lane & 2); oo[r] = (tr + rr == t0 || tr + rr == t1) ? 0x7ffffff0u : lane * 4096 + rr * 1024; }
  const __amdgpu_buffer_rsrc_t ro = __builtin_amdgcn_make_buffer_rsrc(out + tile * 262144 + cg * 8, 0, 262144, 0x00020000);
  PIN(V(yo), V(oo[0]), V(oo[1]), V(oo[2]), V(oo[3]));
  {
    u4v yf; u32 spins = 0;
    do { yf = lds_ldv<u4v>(M_YF + cw * 16); } while (umin(umin(yf.x, yf.y), umin(yf.z, yf.w)) == 0 && (__builtin_amdgcn_s_sleep(SLP_Y), ++spins < SPIN_CAP));
  }
  MEMF();
  __builtin_amdgcn_s_setprio(3);
  {
    f4 y[4];
#pragma unroll
    for (int c = 0; c < 4; c++) y[c] = lds_ld<f4>(yo + c * KREG);
    u32 q[8];
#pragma unroll
    for (int r = 0; r < 4; r += 2) {
      float t[8] = {y[0][r], y[1][r], y[2][r], y[3][r], y[0][r + 1], y[1][r + 1], y[2][r + 1], y[3][r + 1]};
      asm volatile("v_fma_f32 %4, %4, %12, %20\n\tv_fma_f32 %5, %5, %13, %21\n\tv_fma_f32 %6, %6, %14, %22\n\tv_fma_f32 %7, %7, %15, %23\n\tv_fma_f32 %8, %8, %16, %24\n\tv_fma_f32 %9, %9, %17, %25\n\tv_fma_f32 %10, %10, %18, %26\n\tv_fma_f32 %11, %11, %19, %27\n\tv_cvt_pk_bf16_f32 %0, %4, %5\n\tv_cvt_pk_bf16_f32 %1, %6, %7\n\tv_cvt_pk_bf16_f32 %2, %8, %9\n\tv_cvt_pk_bf16_f32 %3, %10, %11"
                   : "=&v"(q[2 * r]), "=&v"(q[2 * r + 1]), "=&v"(q[2 * r + 2]), "=&v"(q[2 * r + 3]), V(t[0]), V(t[1]), V(t[2]), V(t[3]), V(t[4]), V(t[5]), V(t[6]), V(t[7])
                   : "v"(gt[0][r]), "v"(gt[1][r]), "v"(gt[2][r]), "v"(gt[3][r]), "v"(gt[0][r + 1]), "v"(gt[1][r + 1]), "v"(gt[2][r + 1]), "v"(gt[3][r + 1]),
                     "v"(xp[0][r >> 1].x), "v"(xp[1][r >> 1].x), "v"(xp[2][r >> 1].x), "v"(xp[3][r >> 1].x), "v"(xp[0][r >> 1].y), "v"(xp[1][r >> 1].y), "v"(xp[2][r >> 1].y), "v"(xp[3][r >> 1].y));
    }
    asm volatile("s_mov_b64 exec, %8\n\tv_swap_b32 %0, %4\n\tv_swap_b32 %1, %5\n\tv_swap_b32 %2, %6\n\tv_swap_b32 %3, %7\n\ts_mov_b64 exec, -1"
                 : V(q[0]), V(q[1]), V(q[2]), V(q[3]), V(q[4]), V(q[5]), V(q[6]), V(q[7]) : "s"(0xccccccccccccccccull));
#pragma unroll
    for (int r = 0; r < 4; r++) __builtin_amdgcn_raw_buffer_store_b64((u2v){q[2 * r], q[2 * r + 1]}, ro, oo[r], 0, 0);
  }
  };
  if (prod) {
    if (cw < EARLY_W) epi();
    u32 ga = c1_.z + rb, pw = c1_.w + cw * 256, da[8];
#pragma unroll
    for (int t = 0; t < 8; t++) da[t] = (t < 4 ? c2_[t & 3] : c3_[t & 3]) + rb;
    const __amdgpu_buffer_rsrc_t rs = __builtin_amdgcn_make_buffer_rsrc(ws + W_FB + fp * 4096 + cw * 1024, 0, 1024, 0x00020000);
    asm volatile("s_cmp_lt_u32 %1, %2\n\ts_cbranch_scc1 1f\n\ts_waitcnt vmcnt(12)\n1:\n\ts_waitcnt vmcnt(16)" : "+{v210}"(msel) : "s"(cw), "i"(EARLY_W) : "memory", "scc");
    float rs_ = __builtin_amdgcn_rcpf(bh(msel)), mneg = -bl(msel);
    PIN(V(ga), V(pw), V(da[0]), V(da[1]), V(da[2]), V(da[3]), V(da[4]), V(da[5]), V(da[6]), V(da[7]), V(rs_), V(mneg));
    asm volatile("s_cmp_lt_u32 %4, %5\n\ts_cbranch_scc1 1f\n\ts_waitcnt vmcnt(4)\n1:\n\ts_waitcnt vmcnt(8)\n\ts_setprio 3"
                 : "+{v[194:197]}"(xs0), "+{v[198:201]}"(xs1), "+{v[202:205]}"(xs2), "+{v[206:209]}"(xs3) : "s"(cw), "i"(EARLY_W) : "memory", "scc");
    STAMP(9); ZS(1, 1);
    bf8 wf[8];
#pragma unroll
    for (int s = 0; s < 4; s++) { wf[s] = cat(trd(ga + s * 1024), trd(ga + s * 1024 + 128)); wf[4 + s] = cat(trd(ga + 4096 + s * 1024), trd(ga + 4096 + s * 1024 + 128)); }
    __builtin_amdgcn_sched_barrier(0);
    const bf8 xf[4] = {__builtin_bit_cast(bf8, xs0), __builtin_bit_cast(bf8, xs1), __builtin_bit_cast(bf8, xs2), __builtin_bit_cast(bf8, xs3)};
    ZS(1, 2);
#define MG(d, a, b, c) "v_mfma_f32_16x16x32_bf16 " d ", " a ", " b ", " c "\n\t"
#define FD(r) "v_fmac_f32_dpp v" #r ", v" #r ", %[m] quad_perm:[2,2,2,2] row_mask:0xf bank_mask:0xf\n\t"
    asm volatile(MG("v[212:215]", "%0", "%8", "0") MG("v[216:219]", "%4", "%8", "0") MG("v[212:215]", "%1", "%9", "v[212:215]") MG("v[216:219]", "%5", "%9", "v[216:219]")
                 MG("v[212:215]", "%2", "%10", "v[212:215]") MG("v[216:219]", "%6", "%10", "v[216:219]") MG("v[212:215]", "%3", "%11", "v[212:215]") MG("v[216:219]", "%7", "%11", "v[216:219]")
                 "s_nop 7\n\t" FD(212) FD(213) FD(214) FD(215) FD(216) FD(217) FD(218) FD(219)
                 "s_mov_b64 exec, %[e]\n\tds_write_b128 %[p], v[212:215]\n\tds_write_b128 %[p], v[216:219] offset:128\n\ts_mov_b64 exec, -1"
                 :: "v"(wf[0]), "v"(wf[1]), "v"(wf[2]), "v"(wf[3]), "v"(wf[4]), "v"(wf[5]), "v"(wf[6]), "v"(wf[7]), "v"(xf[0]), "v"(xf[1]), "v"(xf[2]), "v"(xf[3]),
                    [m] "v"(mneg), [p] "v"(pw), [e] "s"(0x0003000300030003ull)
                 : "memory", "v212", "v213", "v214", "v215", "v216", "v217", "v218", "v219");
    ZS(1, 4);
    float sg, su;
    {
      u32 n_; float p0, p1, p2, p3, p4, p5, p6, p7;
      asm volatile("s_mov_b32 %[n], 0x40000\n0:\n\tds_read_b32 %2, %[a]\n\tds_read_b32 %3, %[a] offset:128\n\tds_read_b32 %4, %[a] offset:256\n\tds_read_b32 %5, %[a] offset:384\n\t"
                   "ds_read_b32 %6, %[a] offset:512\n\tds_read_b32 %7, %[a] offset:640\n\tds_read_b32 %8, %[a] offset:768\n\tds_read_b32 %9, %[a] offset:896\n\t"
                   "s_sub_u32 %[n], %[n], 1\n\ts_waitcnt lgkmcnt(0)\n\ts_cbranch_scc1 2f\n\tv_max3_u32 %0, %2, %3, %4\n\tv_max3_u32 %1, %5, %6, %7\n\tv_max3_u32 %0, %0, %8, %9\n\t"
                   "v_max_u32 %0, %0, %1\n\tv_cmp_eq_u32 vcc, -1, %0\n\ts_cbranch_vccnz 0b\n"
                   "2:\n\tv_add_f32 %0, %2, %4\n\tv_add_f32 %1, %3, %5\n\tv_add_f32 %6, %6, %8\n\tv_add_f32 %7, %7, %9\n\tv_add_f32 %0, %0, %6\n\tv_add_f32 %1, %1, %7\n\t"
                   "v_mul_f32 %0, %0, %[r]\n\tv_mul_f32 %1, %1, %[r]"
                   : "=&v"(sg), "=&v"(su), "=&v"(p0), "=&v"(p1), "=&v"(p2), "=&v"(p3), "=&v"(p4), "=&v"(p5), "=&v"(p6), "=&v"(p7), [n] "=&s"(n_)
                   : [a] "v"(c5_.x), [r] "v"(rs_) : "memory", "scc", "vcc");
    }
    ZS(1, 5);
    asm volatile("s_cmp_lt_u32 %0, %1\n\ts_cbranch_scc1 1f\n\ts_waitcnt vmcnt(0)\n1:\n\ts_waitcnt vmcnt(4)" :: "s"(cw), "i"(EARLY_W) : "memory", "scc");
    bf4 wdn[8];
#pragma unroll
    for (int t = 0; t < 8; t++) wdn[t] = trd(da[t]);
    __builtin_amdgcn_sched_barrier(0);
    const u32 gu = pk2(sg, su);
    const float hv = rbf(bl(gu) * sigm(bl(gu))) * bh(gu);
    const u2v hb = {pk2(hv, DPPF(hv, 0x124, 0xf)), pk2(DPPF(hv, 0x128, 0xf), DPPF(hv, 0x12C, 0xf))};
    u2v hm[4];
#pragma unroll
    for (int t = 0; t < 4; t++) hm[t] = (u2v){hb.x & c4_[t], hb.y & c4_[t]};
    PIN(V(hm[0]), V(hm[1]), V(hm[2]), V(hm[3]));
    ZS(1, 6);
    f4 y0, y1;
#define MD(d, a, b, c) "v_mfma_f32_16x16x16_bf16 " d ", " a ", " b ", " c "\n\t"
    asm volatile("s_nop 1\n\t" MD("%0", "%2", "%10", "0") MD("%1", "%6", "%10", "0") MD("%0", "%3", "%11", "%0") MD("%1", "%7", "%11", "%1")
                 MD("%0", "%4", "%12", "%0") MD("%1", "%8", "%12", "%1") MD("%0", "%5", "%13", "%0") MD("%1", "%9", "%13", "%1") "s_nop 7"
                 : "=&v"(y0), "=&v"(y1) : "v"(wdn[0]), "v"(wdn[1]), "v"(wdn[2]), "v"(wdn[3]), "v"(wdn[4]), "v"(wdn[5]), "v"(wdn[6]), "v"(wdn[7]),
                   "v"(hm[0]), "v"(hm[1]), "v"(hm[2]), "v"(hm[3]));
    const f4 ys = (lane & 2) ? y1 : y0;
    __builtin_amdgcn_raw_buffer_store_b128((u4v){pk2(ys[0], ys[1]), tag, pk2(ys[2], ys[3]), tag}, rs, c5_.y, 0, 16);
    ZS(1, 7); ZS(4, 6);
    STAMP(10);
  }
  __builtin_amdgcn_s_setprio(1);

  auto prep = [&]() {
    const u32 kk = (lane & 1) ? k2 : k1, tk = ~kk & 2047, ob = kk & 0xffff0000u;
    const u32 tt = tk >> 8;
    const bool mine = (tt - 2 < 4) == (hf != 0);
    const u2v ad = lds_ld<u2v>(lc + 15360 + 8);
    const u32 st_o = tk * 1024 + (lane >> 4) * 2;
    asm volatile("s_cmp_lt_u32 %1, 14\n\ts_cbranch_scc1 1f\n\ts_waitcnt vmcnt(0)\n1:" : "+{v211}"(xe) : "s"(pi) : "memory", "scc");
    spin4(M_UF);
    const u32 ue = lds_ld<u16>(ad.y + tk * 2);
    const float rsg = rbf(sigm(__uint_as_float(ob ^ ((int)ob < 0 ? 0x80000000u : 0xffff0000u))));
    lds_st<u4v>(M_FR + l16, (u4v){__float_as_uint(rsg), ue | (xe << 16), ad.x + (tt < 2 ? tt : tt < 6 ? tt - 2 : tt - 4) * 1024 + (tk & 255) * 4, (mine && (lane & 14) == 0) ? st_o : 0x7ffffff0u});
    MEMF();
    lds_stv<u32>(M_AF, __builtin_amdgcn_ballot_w64(mine) != 0 ? 1u : 2u);
  };
  if (!prod && cw == 3) prep();
  if (!(prod && cw < EARLY_W)) epi();
  if (prod && cw == 0) prep();
  STAMP(12); ZS(4, 1);
  asm volatile("s_waitcnt vmcnt(4)" : V(pfw) :: "memory");
}

extern "C" void kernel_launch(void* const* d_in, const int* in_sizes, int n_in, void* d_out, int out_size,
                              void* d_ws, size_t ws_size, hipStream_t stream) {
  (void)hipFuncSetAttribute((const void*)s4_moe_kernel, hipFuncAttributeMaxDynamicSharedMemorySize, LDS_BYTES);
  s4_moe_kernel<<<256, 512, LDS_BYTES, stream>>>(
      (const u16*)d_in[0], (const u16*)d_in[1], (const u16*)d_in[2], (const u16*)d_in[3], (const u16*)d_in[4],
      (const u16*)d_in[5], (const u16*)d_in[6], (const u16*)d_in[7], (const u16*)d_in[8], (const u16*)d_in[10],
      (const u16*)d_in[11], (const u16*)d_in[12], (const u16*)d_in[13], (u16*)d_out, (char*)d_ws, 0ull);
}
```
